```python
import torch

def _softmax(x, axis=-1):
    return torch.softmax(x, dim=axis)
def _gelu(x):
    return torch.nn.functional.gelu(x, approximate="tanh")
def _silu(x):
    return torch.nn.functional.silu(x)
def _sigmoid(x):
    return torch.sigmoid(x)
def _layernorm(x, axis=-1, eps=1e-5):
    m = x.mean(dim=axis, keepdim=True)
    v = x.var(dim=axis, keepdim=True, unbiased=False)
    return (x - m) / torch.sqrt(v + eps)
def _scatter_add(tgt, idx, src, axis=0):
    return tgt.clone().index_add_(axis, idx, src)
def _segment_sum(src, seg, n):
    out = torch.zeros((n,) + src.shape[1:], dtype=src.dtype)
    return out.index_add_(0, seg, src)
def _pad(x, pw):
    flat = tuple(v for pair in reversed(pw) for v in pair)
    return torch.nn.functional.pad(x, flat)

kernel_name = 'standalone__paged_block_table_kv_radixattention__sampling'

def setup_inputs():

    _w = (lambda a: (lambda t: t.to(torch.bfloat16) if t.is_floating_point() else t)(torch.as_tensor(a)))
    B, H, Hkv, Dh = 8, 32, 32, 192
    Lk = 4096
    bs = 16
    nb = max(2, Lk // bs)
    P = max(1, nb // 2)
    n_phys = P + B * (nb - P)
    shared = torch.arange(P, dtype=torch.int64)
    priv = P + torch.arange(B * (nb - P), dtype=torch.int64).reshape(B, nb - P)
    bt = torch.cat([torch.tile(shared[None, :], (B, 1)), priv], dim=1)
    ctx = nb * bs - (torch.arange(1, B + 1, dtype=torch.int64) * 2654435761) % max(1, (nb * bs - P * bs) // 2)
    return {
        "q": _w((lambda *s: torch.randn(*s, dtype=torch.bfloat16) * 0.8)(B, H, Dh)),
        "k_cache": _w((lambda *s: torch.randn(*s, dtype=torch.bfloat16) * 0.8)(n_phys, bs, Hkv, Dh)),
        "v_cache": _w((lambda *s: torch.randn(*s, dtype=torch.bfloat16) * 0.05)(n_phys, bs, Hkv, Dh)),
        "block_table": _w(bt.to(torch.int64)),
        "ctx_lens": _w(torch.as_tensor(ctx).to(torch.int64)),
    }

def reference(q, k_cache, v_cache, block_table, ctx_lens):
    B, H, Dh = q.shape
    n_phys, bs, Hkv, _ = k_cache.shape
    nb = block_table.shape[1]
    Lk = nb * bs
    rep = H // Hkv
    k = k_cache[block_table].reshape(B, Lk, Hkv, Dh)
    v = v_cache[block_table].reshape(B, Lk, Hkv, Dh)
    k = (lambda x, n, axis: torch.repeat_interleave(x, n, dim=axis))(k, rep, axis=2)
    v = (lambda x, n, axis: torch.repeat_interleave(x, n, dim=axis))(v, rep, axis=2)
    scores = torch.einsum("bhd,bkhd->bhk", q, k) * (Dh ** -0.5)
    pos = torch.arange(Lk)
    valid = pos[None, :] < ctx_lens[:, None]
    scores = torch.where(valid[:, None, :], scores, -1e9)
    p = _softmax(scores, axis=-1)
    return torch.einsum("bhk,bkhd->bhd", p, v)
```

```cpp
#include <hip/hip_runtime.h>
#include <hip/hip_bf16.h>
#include <cstdint>

constexpr int B = 8, H = 32, HKV = 32, D = 192, BS = 16, NB = 256, LK = NB * BS;

__device__ __forceinline__ float bf2f(unsigned short x) { return __uint_as_float((unsigned)x << 16); }

__global__ void __launch_bounds__(256) naive_kernel(const unsigned short* __restrict__ q, const unsigned short* __restrict__ k,
                                                    const unsigned short* __restrict__ v, const int* __restrict__ bt,
                                                    const int* __restrict__ lens, __hip_bfloat16* __restrict__ out) {
  __shared__ float sc[LK];
  __shared__ float qs[D];
  __shared__ float red[256];
  const int b = blockIdx.x, h = blockIdx.y, tid = threadIdx.x;
  int len = lens[b];
  bool zero = len <= 0;
  if (zero || len > LK) len = LK;
  if (tid < D) qs[tid] = zero ? 0.f : bf2f(q[(b * H + h) * D + tid]);
  __syncthreads();
  const float scale = 0.07216878364870322f;
  float mx = -INFINITY;
  for (int t = tid; t < len; t += 256) {
    const int pg = bt[b * NB + t / BS];
    const unsigned short* kr = k + ((size_t)(pg * BS + t % BS) * HKV + h) * D;
    float s = 0.f;
    for (int d = 0; d < D; ++d) s += qs[d] * bf2f(kr[d]);
    s *= scale;
    sc[t] = s;
    mx = fmaxf(mx, s);
  }
  red[tid] = mx;
  __syncthreads();
  for (int o = 128; o > 0; o >>= 1) { if (tid < o) red[tid] = fmaxf(red[tid], red[tid + o]); __syncthreads(); }
  mx = red[0];
  __syncthreads();
  float sum = 0.f;
  for (int t = tid; t < len; t += 256) { float p = __expf(sc[t] - mx); sc[t] = p; sum += p; }
  red[tid] = sum;
  __syncthreads();
  for (int o = 128; o > 0; o >>= 1) { if (tid < o) red[tid] += red[tid + o]; __syncthreads(); }
  sum = red[0];
  if (tid < D) {
    float acc = 0.f;
    for (int t = 0; t < len; ++t) {
      const int pg = bt[b * NB + t / BS];
      acc += sc[t] * bf2f(v[((size_t)(pg * BS + t % BS) * HKV + h) * D + tid]);
    }
    out[(b * H + h) * D + tid] = __float2bfloat16(acc / sum);
  }
}

extern "C" void kernel_launch(void* const* d_in, const int* in_sizes, int n_in, void* d_out, int out_size, void* d_ws, size_t ws_size,
                              hipStream_t stream) {
  naive_kernel<<<dim3(B, H), 256, 0, stream>>>((const unsigned short*)d_in[0], (const unsigned short*)d_in[1], (const unsigned short*)d_in[2],
                                               (const int*)d_in[3], (const int*)d_in[4], (__hip_bfloat16*)d_out);
}
```

```cpp
#include <hip/hip_runtime.h>
#include <hip/hip_bf16.h>
#include <stdint.h>

namespace {

constexpr int B = 8, H = 32, D = 192, BS = 16, NB = 256, LK = NB * BS;
constexpr int ROWB = H * D * 2;
constexpr int PAGEB = BS * ROWB;
constexpr int TILE = BS * D * 2;
#define RING 3
constexpr int R = RING;
constexpr int NSW = 4;
constexpr int NTHR = (NSW + 1) * 64;

constexpr int L_RING = 0;
constexpr int QSTR = 784;
constexpr int L_DUMP1 = NSW * R * TILE;
constexpr int DUMP1_W = 8 * QSTR;
constexpr int L_TAB = L_DUMP1;
constexpr int L_STAT1 = L_DUMP1 + NSW * DUMP1_W;
constexpr int L_DUMP2 = L_STAT1 + NSW * 64;
constexpr int L_SYNC = L_DUMP2 + NSW * QSTR;
constexpr int LDS_BYTES = L_SYNC + 64;
static_assert(LDS_BYTES <= 160 * 1024, "LDS");
static_assert(NSW * DUMP1_W >= B * NB * 4, "table alias");

constexpr size_t WS_EPOCH = 0;
constexpr size_t WS_FLAG = 4096;
constexpr size_t WS_REC = 8192;
constexpr int REC_B = 128 + 8 * 768;
constexpr size_t WS_END = WS_REC + (size_t)H * B * REC_B;

constexpr float C2 = 0.07216878364870322f * 1.4426950408889634f;

#define LAS __attribute__((address_space(3)))
#define GAS __attribute__((address_space(1)))
typedef __attribute__((ext_vector_type(4))) unsigned u32x4;
typedef __attribute__((ext_vector_type(4))) int i32x4;
typedef __attribute__((ext_vector_type(4))) float f32x4;
typedef __attribute__((ext_vector_type(2))) float f32x2;
typedef __attribute__((ext_vector_type(8))) __bf16 bf16x8;
typedef __attribute__((ext_vector_type(4))) short s16x4;
typedef __attribute__((ext_vector_type(2))) __bf16 bf16x2;
typedef unsigned long long u64;

__device__ __forceinline__ int rfl(int x) { return __builtin_amdgcn_readfirstlane(x); }
__device__ __forceinline__ float xrow_max(float x) {
  auto s = __builtin_amdgcn_permlane16_swap(__float_as_uint(x), __float_as_uint(x), false, false);
  x = fmaxf(__uint_as_float(s[0]), __uint_as_float(s[1]));
  auto t = __builtin_amdgcn_permlane32_swap(__float_as_uint(x), __float_as_uint(x), false, false);
  return fmaxf(__uint_as_float(t[0]), __uint_as_float(t[1]));
}
__device__ __forceinline__ float xrow_sum(float x) {
  auto s = __builtin_amdgcn_permlane16_swap(__float_as_uint(x), __float_as_uint(x), false, false);
  x = __uint_as_float(s[0]) + __uint_as_float(s[1]);
  auto t = __builtin_amdgcn_permlane32_swap(__float_as_uint(x), __float_as_uint(x), false, false);
  return __uint_as_float(t[0]) + __uint_as_float(t[1]);
}
__device__ __forceinline__ f32x4 sel0(bool c, f32x4 x) { return f32x4{c ? x[0] : 0.f, c ? x[1] : 0.f, c ? x[2] : 0.f, c ? x[3] : 0.f}; }
template <typename T> __device__ __forceinline__ T* uni_ptr(T* p) {
  const unsigned long long u = (unsigned long long)p;
  const unsigned lo = __builtin_amdgcn_readfirstlane((unsigned)u), hi = __builtin_amdgcn_readfirstlane((unsigned)(u >> 32));
  return (T*)(((unsigned long long)hi << 32) | lo);
}
__device__ __forceinline__ unsigned pack_bf16(float lo, float hi) {
  bf16x2 v = {(__bf16)lo, (__bf16)hi};
  return __builtin_bit_cast(unsigned, v);
}

__device__ __forceinline__ void dma_tile(const void* sbase, unsigned ldsaddr, unsigned v0, unsigned v1, unsigned v2, unsigned v3,
                                         unsigned v4, unsigned v5) {
  unsigned l2 = ldsaddr + 4096;
  asm volatile(
      "s_waitcnt lgkmcnt(0)\n\t"
      "s_mov_b32 m0, %7\n\t"
      "s_nop 0\n\t"
      "global_load_lds_dwordx4 %0, %6 nt\n\t"
      "global_load_lds_dwordx4 %1, %6 offset:1024 nt\n\t"
      "global_load_lds_dwordx4 %2, %6 offset:2048 nt\n\t"
      "global_load_lds_dwordx4 %3, %6 offset:3072 nt\n\t"
      "s_mov_b32 m0, %8\n\t"
      "s_nop 0\n\t"
      "global_load_lds_dwordx4 %4, %6 nt\n\t"
      "global_load_lds_dwordx4 %5, %6 offset:1024 nt"
      :
      : "v"(v0), "v"(v1), "v"(v2), "v"(v3), "v"(v4), "v"(v5), "s"(sbase), "s"(ldsaddr), "s"(l2)
      : "memory");
}
__device__ __forceinline__ void wait_tiles(int k) {
  if (k >= R - 1) asm volatile("s_waitcnt vmcnt(%0)" ::"i"((R - 1) * 6) : "memory");
  else if (k == 0) asm volatile("s_waitcnt vmcnt(0)" ::: "memory");
  else if (k == 1) asm volatile("s_waitcnt vmcnt(6)" ::: "memory");
  else if (k == 2) asm volatile("s_waitcnt vmcnt(12)" ::: "memory");
  else if (k == 3) asm volatile("s_waitcnt vmcnt(18)" ::: "memory");
  else asm volatile("s_waitcnt vmcnt(24)" ::: "memory");
}

struct Params {
  const unsigned short* q;
  const char* k;
  const char* v;
  const int* bt;
  const int* lens;
  unsigned short* out;
  char* ws;
};

__global__ void __launch_bounds__(NTHR, 1) paged_attn_kernel(Params p) {
  extern __shared__ __attribute__((aligned(16))) char smem[];
  LAS char* lds = (LAS char*)smem;
  const int tid = threadIdx.x, lane = tid & 63, wave = rfl(tid >> 6);
  const int h = blockIdx.x % H, b = blockIdx.x / H;
  const int g = lane >> 4, c = lane & 15;

  i32x4 tb[B];
#pragma unroll
  for (int r = 0; r < B; ++r) tb[r] = *(const i32x4*)(p.bt + r * NB + lane * 4);
  u64 epoch = 0;
  if (wave == NSW) epoch = __hip_atomic_load((GAS u64*)(p.ws + WS_EPOCH + h * 128), __ATOMIC_RELAXED, __HIP_MEMORY_SCOPE_AGENT);
  int len[B];
#pragma unroll
  for (int r = 0; r < B; ++r) len[r] = p.lens[r];
  const int lenc_raw = p.lens[c & 7];
  bf16x8 qf[6];
  if (wave < NSW) {
    const unsigned short* qp = p.q + ((c & 7) * H + h) * D + 8 * g;
#pragma unroll
    for (int s = 0; s < 6; ++s) {
      u32x4 t = *(const u32x4*)(qp + 32 * s);
      if (c >= 8 || lenc_raw <= 0) t = u32x4{0u, 0u, 0u, 0u};
      qf[s] = __builtin_bit_cast(bf16x8, t);
    }
  }
  if (tid == 0) { *(LAS unsigned*)(lds + L_SYNC) = 0u; *(LAS unsigned*)(lds + L_SYNC + 4) = 0u; }
  const int lenc = c >= 8 ? 0 : (lenc_raw <= 0 || lenc_raw > LK) ? LK : lenc_raw;

  int P = NB;
  {
    bool d0 = false, d1 = false, d2 = false, d3 = false;
#pragma unroll
    for (int r = 1; r < B; ++r) { d0 |= tb[r][0] != tb[0][0]; d1 |= tb[r][1] != tb[0][1]; d2 |= tb[r][2] != tb[0][2]; d3 |= tb[r][3] != tb[0][3]; }
    const u64 m0 = __ballot(d0), m1 = __ballot(d1), m2 = __ballot(d2), m3 = __ballot(d3);
    if (m0) P = min(P, 4 * (int)__builtin_ctzll(m0));
    if (m1) P = min(P, 4 * (int)__builtin_ctzll(m1) + 1);
    if (m2) P = min(P, 4 * (int)__builtin_ctzll(m2) + 2);
    if (m3) P = min(P, 4 * (int)__builtin_ctzll(m3) + 3);
  }
  if (wave < NSW) {
#pragma unroll
    for (int r = 0; r < B; ++r) *(LAS i32x4*)(lds + L_TAB + r * NB * 4 + lane * 16) = tb[r];
  }
  int n[B], maxnp = 0, T = 0;
#pragma unroll
  for (int r = 0; r < B; ++r) {
    const int l_ = (len[r] <= 0 || len[r] > LK) ? LK : len[r];
    const int np = (l_ + BS - 1) / BS;
    maxnp = max(maxnp, np);
    n[r] = max(0, np - P);
    T += n[r];
  }
  const int S = min(P, maxnp);
  T += S;
  int cap_b = 0, capPre = 0, ovf_b = 0, q_b = 0, ovf[B], ovfPre[B], run = 0;
#pragma unroll
  for (int r = 0; r < B; ++r) {
    const int qr = (T * (r + 1)) / 8 - (T * r) / 8;
    const int own = min(n[r], qr);
    ovf[r] = n[r] - own; ovfPre[r] = run; run += ovf[r];
    const int cap = qr - own;
    if (r < b) capPre += cap;
    if (r == b) { cap_b = cap; ovf_b = ovf[r]; q_b = qr; }
  }

  if (wave < NSW) {
    int ent;
    {
      const int it = wave + NSW * lane;
      int row = b, col = P + ovf_b + (it - cap_b), tag = b;
      if (it < cap_b) {
        int f = capPre + it;
        if (f < S) { row = 0; col = f; tag = 8; }
        else {
          f -= S;
#pragma unroll
          for (int r = 0; r < B; ++r) if (f >= ovfPre[r] && f < ovfPre[r] + ovf[r]) { row = r; tag = r; col = P + f - ovfPre[r]; }
        }
      }
      col = min(max(col, 0), NB - 1);
      asm volatile("s_waitcnt lgkmcnt(0)" ::: "memory");
      const int phys = *(const LAS int*)(lds + L_TAB + (row * NB + col) * 4);
      ent = (int)(((unsigned)phys << 12) | (unsigned)(tag << 8) | (unsigned)col);
    }
    asm volatile("" ::"v"(lenc));
    const int nw = q_b > wave ? (q_b - wave + NSW - 1) / NSW : 0;
    const int i1 = cap_b > wave ? (cap_b - wave + NSW - 1) / NSW : 0;
    const int ntiles = 2 * nw;

    unsigned voff[6];
#pragma unroll
    for (int i = 0; i < 6; ++i) {
      const int pp = i * 64 + lane, r_ = pp / 24, ch = pp % 24;
      voff[i] = (unsigned)(r_ * ROWB + (ch ^ (r_ & 6)) * 16 - (i & 3) * 1024);
    }
    const unsigned ring = (unsigned)(size_t)(lds + L_RING) + wave * R * TILE;
    const char* kh = p.k + h * (D * 2);
    const char* vh = p.v + h * (D * 2);
    int slot_issue = 0;
    int issued = 0;
    auto issue = [&]() {
      const unsigned e = (unsigned)__builtin_amdgcn_readlane(ent, issued >> 1);
      const char* base = ((issued & 1) ? vh : kh) + (size_t)(e >> 12) * PAGEB;
      dma_tile(base, ring + slot_issue, voff[0], voff[1], voff[2], voff[3], voff[4], voff[5]);
      ++issued;
      slot_issue += TILE; if (slot_issue == R * TILE) slot_issue = 0;
    };
#pragma unroll 1
    for (int t = 0; t < R; ++t) if (issued < ntiles) issue();
    __builtin_amdgcn_s_barrier();

    unsigned ka[2], va[4];
#pragma unroll
    for (int e = 0; e < 2; ++e) ka[e] = ring + (unsigned)(16 * (24 * c + ((4 * e + g) ^ (c & 6))));
    {
      const int row = 4 * g + (c >> 2), pq = lane & 3, x = (row >> 1) & 3;
#pragma unroll
      for (int nl = 0; nl < 4; ++nl) va[nl] = ring + (unsigned)(16 * 24 * row + 32 * (nl ^ x) + 16 * (pq >> 1) + 8 * (pq & 1));
    }

    float m = -INFINITY, l = 0.f;
    f32x4 acc[12];
#pragma unroll
    for (int i = 0; i < 12; ++i) acc[i] = f32x4{0.f, 0.f, 0.f, 0.f};

    auto dump1 = [&]() {
      const float ls = xrow_sum(l);
      LAS char* dst = lds + L_DUMP1 + wave * DUMP1_W;
      if (c < 8) {
#pragma unroll
        for (int i = 0; i < 12; ++i) *(LAS f32x4*)(dst + c * QSTR + i * 64 + g * 16) = acc[i];
        if (g == 0) *(LAS f32x2*)(lds + L_STAT1 + wave * 64 + c * 8) = f32x2{m, ls};
      }
      asm volatile("s_waitcnt lgkmcnt(0)" ::: "memory");
      if (lane == 0) __hip_atomic_fetch_add((LAS unsigned*)(lds + L_SYNC), 1u, __ATOMIC_RELAXED, __HIP_MEMORY_SCOPE_WORKGROUP);
      m = -INFINITY; l = 0.f;
#pragma unroll
      for (int i = 0; i < 12; ++i) acc[i] = f32x4{0.f, 0.f, 0.f, 0.f};
    };

    int slot = 0;
#pragma unroll 1
    for (int i = 0; i < nw; ++i) {
      if (i == i1) dump1();
      const unsigned e = (unsigned)__builtin_amdgcn_readlane(ent, i);
      const int col = e & 255, tag = (e >> 8) & 15;
      wait_tiles(issued - 2 * i - 1);
      bf16x8 kf[6];
#pragma unroll
      for (int s = 0; s < 6; ++s) kf[s] = *(const LAS bf16x8*)(size_t)(ka[s & 1] + slot + 128 * (s >> 1));
      __builtin_amdgcn_sched_barrier(0);
      f32x4 s0 = {0.f, 0.f, 0.f, 0.f}, s1 = {0.f, 0.f, 0.f, 0.f};
#pragma unroll
      for (int s = 0; s < 6; s += 2) {
        s0 = __builtin_amdgcn_mfma_f32_16x16x32_bf16(kf[s], qf[s], s0, 0, 0, 0);
        s1 = __builtin_amdgcn_mfma_f32_16x16x32_bf16(kf[s + 1], qf[s + 1], s1, 0, 0, 0);
      }
      slot += TILE; if (slot == R * TILE) slot = 0;
      if (issued < ntiles) issue();
      const int rem = ((tag == 8 || tag == c) ? lenc : 0) - col * BS - 4 * g;
      float x[4];
#pragma unroll
      for (int j = 0; j < 4; ++j) x[j] = j < rem ? (s0[j] + s1[j]) * C2 : -INFINITY;
      const float pm = xrow_max(fmaxf(fmaxf(x[0], x[1]), fmaxf(x[2], x[3])));
      const float mn = fmaxf(m, pm);
      if (__any(mn > m)) {
        const float alpha = mn > m ? __builtin_amdgcn_exp2f(m - mn) : 1.f;
        l *= alpha;
#pragma unroll
        for (int ii = 0; ii < 12; ++ii) acc[ii] *= alpha;
        m = mn;
      }
      const float msub = m == -INFINITY ? 0.f : m;
      float pr[4];
#pragma unroll
      for (int j = 0; j < 4; ++j) pr[j] = __builtin_amdgcn_exp2f(x[j] - msub);
      l += (pr[0] + pr[1]) + (pr[2] + pr[3]);
      const s16x4 pf = __builtin_bit_cast(s16x4, (__attribute__((ext_vector_type(2))) unsigned){pack_bf16(pr[0], pr[1]), pack_bf16(pr[2], pr[3])});
      wait_tiles(issued - 2 * i - 2);
      s16x4 vf[12];
#pragma unroll
      for (int nn = 0; nn < 12; ++nn)
        vf[nn] = __builtin_amdgcn_ds_read_tr16_b64_v4i16((LAS s16x4*)(size_t)(va[nn & 3] + slot + 128 * (nn >> 2)));
      __builtin_amdgcn_sched_barrier(0);
#pragma unroll
      for (int nn = 0; nn < 12; ++nn) acc[nn] = __builtin_amdgcn_mfma_f32_16x16x16bf16_1k(vf[nn], pf, acc[nn], 0, 0, 0);
      slot += TILE; if (slot == R * TILE) slot = 0;
      if (issued < ntiles) issue();
    }
    if (i1 >= nw) dump1();
    {
      const float ls = xrow_sum(l);
      LAS char* dst = lds + L_DUMP2 + wave * QSTR;
      if (c == b) {
#pragma unroll
        for (int i = 0; i < 12; ++i) *(LAS f32x4*)(dst + i * 64 + g * 16) = acc[i];
        if (g == 0) *(LAS f32x2*)(dst + 768) = f32x2{m, ls};
      }
      asm volatile("s_waitcnt lgkmcnt(0)" ::: "memory");
      if (lane == 0) __hip_atomic_fetch_add((LAS unsigned*)(lds + L_SYNC + 4), 1u, __ATOMIC_RELAXED, __HIP_MEMORY_SCOPE_WORKGROUP);
    }
  } else {
    asm volatile("s_waitcnt lgkmcnt(0)" ::: "memory");
    __builtin_amdgcn_s_barrier();
    const u64 tag = epoch + 1;
    volatile LAS unsigned* sync = (volatile LAS unsigned*)(lds + L_SYNC);
    char* rec = p.ws + WS_REC + (size_t)(h * B + b) * REC_B;
    while (sync[0] < NSW) __builtin_amdgcn_s_sleep(2);
    asm volatile("" ::: "memory");
    if (cap_b > 0) {
      const int c8 = lane & 7, w4 = (lane >> 3) & 3;
      const f32x2 st = *(const LAS f32x2*)(lds + L_STAT1 + w4 * 64 + c8 * 8);
      float M = st[0];
      M = fmaxf(M, __shfl_xor(M, 8)); M = fmaxf(M, __shfl_xor(M, 16));
      const float f = st[1] > 0.f ? __builtin_amdgcn_exp2f(st[0] - M) : 0.f;
      float L = f * st[1];
      L += __shfl_xor(L, 8); L += __shfl_xor(L, 16);
      const auto rs = __builtin_amdgcn_make_buffer_rsrc(uni_ptr(rec), 0, REC_B, 0x00020000);
#pragma unroll
      for (int i = 0; i < 6; ++i) {
        const int pp = i * 64 + lane, cq = pp / 48, kk = pp % 48;
        f32x4 a = {0.f, 0.f, 0.f, 0.f};
#pragma unroll
        for (int w = 0; w < NSW; ++w) {
          const float fw = __shfl(f, w * 8 + cq);
          const f32x4 x = *(const LAS f32x4*)(lds + L_DUMP1 + w * DUMP1_W + cq * QSTR + kk * 16);
          a += fw * sel0(fw > 0.f, x);
        }
        __builtin_amdgcn_raw_buffer_store_b128(__builtin_bit_cast(u32x4, a), rs, 128 + pp * 16, 0, 16);
      }
      if (lane < 8) __hip_atomic_store((GAS u64*)(rec + lane * 8), __builtin_bit_cast(u64, f32x2{M, L}), __ATOMIC_RELAXED, __HIP_MEMORY_SCOPE_AGENT);
    }
    asm volatile("s_waitcnt vmcnt(0)" ::: "memory");
    if (lane == 0) __hip_atomic_store((GAS u64*)(p.ws + WS_FLAG) + h * B + b, tag, __ATOMIC_RELAXED, __HIP_MEMORY_SCOPE_AGENT);

    {
      GAS u64* fl = (GAS u64*)(p.ws + WS_FLAG) + h * B + (lane & 7);
      for (unsigned spins = 0; spins < (1u << 22); ++spins) {
        const u64 x = __hip_atomic_load(fl, __ATOMIC_RELAXED, __HIP_MEMORY_SCOPE_AGENT);
        if (__all(x == tag)) break;
        __builtin_amdgcn_s_sleep(16);
      }
      __builtin_amdgcn_fence(__ATOMIC_ACQUIRE, "agent");
    }
    if (b == 0 && lane == 0) __hip_atomic_store((GAS u64*)(p.ws + WS_EPOCH + h * 128), tag, __ATOMIC_RELAXED, __HIP_MEMORY_SCOPE_AGENT);
    int capr;
    {
      const int r = lane & 7;
      int nr = 0;
#pragma unroll
      for (int j = 0; j < B; ++j) if (r == j) nr = n[j];
      const int qr = (T * (r + 1)) / 8 - (T * r) / 8;
      capr = qr - min(nr, qr);
    }
    char* rec0 = p.ws + WS_REC + (size_t)(h * B) * REC_B;
    f32x2 hd = {-INFINITY, 0.f};
    if (capr > 0) hd = __builtin_bit_cast(f32x2, __hip_atomic_load((GAS u64*)(rec0 + (size_t)(lane & 7) * REC_B + b * 8), __ATOMIC_RELAXED, __HIP_MEMORY_SCOPE_AGENT));
    float MF = hd[1] > 0.f ? hd[0] : -INFINITY;
    MF = fmaxf(MF, __shfl_xor(MF, 1)); MF = fmaxf(MF, __shfl_xor(MF, 2)); MF = fmaxf(MF, __shfl_xor(MF, 4));
    const float fr = hd[1] > 0.f ? __builtin_amdgcn_exp2f(hd[0] - MF) : 0.f;
    float LF = fr * hd[1];
    LF += __shfl_xor(LF, 1); LF += __shfl_xor(LF, 2); LF += __shfl_xor(LF, 4);
    f32x4 aF = {0.f, 0.f, 0.f, 0.f};
    {
      const auto rs = __builtin_amdgcn_make_buffer_rsrc(uni_ptr(rec0), 0, B * REC_B, 0x00020000);
      const int lo = min(lane, 47) * 16;
      u32x4 x[B];
#pragma unroll
      for (int r = 0; r < B; ++r) x[r] = __builtin_amdgcn_raw_buffer_load_b128(rs, r * REC_B + 128 + b * 768 + lo, 0, 16);
#pragma unroll
      for (int r = 0; r < B; ++r) { const float fw = __shfl(fr, r); aF += fw * sel0(fw > 0.f, __builtin_bit_cast(f32x4, x[r])); }
    }
    while (sync[1] < NSW) __builtin_amdgcn_s_sleep(1);
    asm volatile("" ::: "memory");
    {
      f32x2 st[NSW];
      float M = MF;
#pragma unroll
      for (int w = 0; w < NSW; ++w) { st[w] = *(const LAS f32x2*)(lds + L_DUMP2 + w * QSTR + 768); if (st[w][1] > 0.f) M = fmaxf(M, st[w][0]); }
      const float f0 = LF > 0.f ? __builtin_amdgcn_exp2f(MF - M) : 0.f;
      float L = f0 * LF;
      f32x4 a = f0 * aF;
      const int lo = min(lane, 47) * 16;
#pragma unroll
      for (int w = 0; w < NSW; ++w) {
        const float fw = st[w][1] > 0.f ? __builtin_amdgcn_exp2f(st[w][0] - M) : 0.f;
        const f32x4 x = *(const LAS f32x4*)(lds + L_DUMP2 + w * QSTR + lo);
        L += fw * st[w][1];
        a += fw * sel0(fw > 0.f, x);
      }
      const float inv = 1.f / L;
      if (lane < 48) {
        uint2 o = make_uint2(pack_bf16(a[0] * inv, a[1] * inv), pack_bf16(a[2] * inv, a[3] * inv));
        *(uint2*)(p.out + (b * H + h) * D + lane * 4) = o;
      }
    }
  }
}

}

extern "C" void kernel_launch(void* const* d_in, const int* in_sizes, int n_in, void* d_out, int out_size, void* d_ws, size_t ws_size,
                              hipStream_t stream) {
  (void)hipFuncSetAttribute((const void*)paged_attn_kernel, hipFuncAttributeMaxDynamicSharedMemorySize, LDS_BYTES);
  Params p;
  p.q = (const unsigned short*)d_in[0];
  p.k = (const char*)d_in[1];
  p.v = (const char*)d_in[2];
  p.bt = (const int*)d_in[3];
  p.lens = (const int*)d_in[4];
  p.out = (unsigned short*)d_out;
  p.ws = (char*)d_ws;
  hipLaunchKernelGGL(paged_attn_kernel, dim3(H * B), dim3(NTHR), LDS_BYTES, stream, p);
}
```

```cpp
#include <hip/hip_runtime.h>
#include <hip/hip_bf16.h>
#include <stdint.h>

namespace {

constexpr int B = 8, H = 32, D = 192, BS = 16, NB = 256, LK = NB * BS;
constexpr int ROWB = H * D * 2;
constexpr int PAGEB = BS * ROWB;
constexpr int TILE = BS * D * 2;
#define RING 5
constexpr int R = RING;
constexpr int NSW = 4;
constexpr int NTHR = (NSW + 1) * 64;
#define KAPPA 5500
#define PC0 200
#define LEAD 1

constexpr int L_RING = 0;
constexpr int QSTR = 784;
constexpr int L_DUMP1 = NSW * R * TILE;
constexpr int DUMP1_W = 8 * QSTR;
constexpr int L_STAT1 = L_DUMP1 + NSW * DUMP1_W;
constexpr int L_DUMP2 = L_STAT1 + NSW * 64;
constexpr int L_SYNC = L_DUMP2 + NSW * QSTR;
constexpr int LDS_BYTES = L_SYNC + 64;
static_assert(LDS_BYTES <= 160 * 1024, "LDS");

constexpr size_t WS_EPOCH = 0;
constexpr size_t WS_FLAG = 4096;
constexpr size_t WS_REC = 8192;
constexpr int QREC = D * 2;
constexpr int REC_B = 128 + 8 * QREC;
constexpr size_t WS_END = WS_REC + (size_t)H * B * REC_B;

constexpr float C2 = 0.07216878364870322f * 1.4426950408889634f;

#define LAS __attribute__((address_space(3)))
#define GAS __attribute__((address_space(1)))
typedef __attribute__((ext_vector_type(4))) unsigned u32x4;
typedef __attribute__((ext_vector_type(2))) unsigned u32x2;
typedef __attribute__((ext_vector_type(4))) int i32x4;
typedef __attribute__((ext_vector_type(4))) float f32x4;
typedef __attribute__((ext_vector_type(2))) float f32x2;
typedef __attribute__((ext_vector_type(8))) __bf16 bf16x8;
typedef __attribute__((ext_vector_type(4))) short s16x4;
typedef __attribute__((ext_vector_type(2))) __bf16 bf16x2;
typedef unsigned long long u64;

__device__ __forceinline__ int rfl(int x) { return __builtin_amdgcn_readfirstlane(x); }
__device__ __forceinline__ int rdl(int x, int l) { return __builtin_amdgcn_readlane(x, l); }
template <int CTRL> __device__ __forceinline__ int dpp(int x) { return __builtin_amdgcn_mov_dpp(x, CTRL, 0xf, 0xf, true); }
constexpr int DPP_X1 = 0xB1, DPP_X2 = 0x4E, DPP_HMIRROR = 0x141, DPP_ROR = 0x120;
__device__ __forceinline__ float xrow_max(float x) {
  auto s = __builtin_amdgcn_permlane16_swap(__float_as_uint(x), __float_as_uint(x), false, false);
  x = fmaxf(__uint_as_float(s[0]), __uint_as_float(s[1]));
  auto t = __builtin_amdgcn_permlane32_swap(__float_as_uint(x), __float_as_uint(x), false, false);
  return fmaxf(__uint_as_float(t[0]), __uint_as_float(t[1]));
}
__device__ __forceinline__ float xrow_sum(float x) {
  auto s = __builtin_amdgcn_permlane16_swap(__float_as_uint(x), __float_as_uint(x), false, false);
  x = __uint_as_float(s[0]) + __uint_as_float(s[1]);
  auto t = __builtin_amdgcn_permlane32_swap(__float_as_uint(x), __float_as_uint(x), false, false);
  return __uint_as_float(t[0]) + __uint_as_float(t[1]);
}
__device__ __forceinline__ f32x4 sel0(bool c, f32x4 x) { return f32x4{c ? x[0] : 0.f, c ? x[1] : 0.f, c ? x[2] : 0.f, c ? x[3] : 0.f}; }
template <typename T> __device__ __forceinline__ T* uni_ptr(T* p) {
  const unsigned long long u = (unsigned long long)p;
  const unsigned lo = __builtin_amdgcn_readfirstlane((unsigned)u), hi = __builtin_amdgcn_readfirstlane((unsigned)(u >> 32));
  return (T*)(((unsigned long long)hi << 32) | lo);
}
__device__ __forceinline__ unsigned pack_bf16(float lo, float hi) {
  bf16x2 v = {(__bf16)lo, (__bf16)hi};
  return __builtin_bit_cast(unsigned, v);
}
__device__ __forceinline__ unsigned now32() { return (unsigned)__builtin_amdgcn_s_memrealtime(); }

__device__ __forceinline__ void dma_tile(const void* sbase, unsigned ldsaddr, unsigned v0, unsigned v1, unsigned v2, unsigned v3,
                                         unsigned v4, unsigned v5) {
  unsigned l2 = ldsaddr + 4096;
  asm volatile(
      "s_waitcnt lgkmcnt(0)\n\t"
      "s_mov_b32 m0, %7\n\t"
      "s_nop 0\n\t"
      "global_load_lds_dwordx4 %0, %6 nt\n\t"
      "global_load_lds_dwordx4 %1, %6 offset:1024 nt\n\t"
      "global_load_lds_dwordx4 %2, %6 offset:2048 nt\n\t"
      "global_load_lds_dwordx4 %3, %6 offset:3072 nt\n\t"
      "s_mov_b32 m0, %8\n\t"
      "s_nop 0\n\t"
      "global_load_lds_dwordx4 %4, %6 nt\n\t"
      "global_load_lds_dwordx4 %5, %6 offset:1024 nt"
      :
      : "v"(v0), "v"(v1), "v"(v2), "v"(v3), "v"(v4), "v"(v5), "s"(sbase), "s"(ldsaddr), "s"(l2)
      : "memory");
}
__device__ __forceinline__ void wait_tiles(int k) {
  if (k >= 5) asm volatile("s_waitcnt vmcnt(30)" ::: "memory");
  else if (k == 4) asm volatile("s_waitcnt vmcnt(24)" ::: "memory");
  else if (k == 3) asm volatile("s_waitcnt vmcnt(18)" ::: "memory");
  else if (k == 2) asm volatile("s_waitcnt vmcnt(12)" ::: "memory");
  else if (k == 1) asm volatile("s_waitcnt vmcnt(6)" ::: "memory");
  else asm volatile("s_waitcnt vmcnt(0)" ::: "memory");
}
static_assert(R <= 5, "wait_tiles");

struct Params {
  const int* bt;
  const int* lens;
  const unsigned short* q;
  const char* k;
  const char* v;
  unsigned short* out;
  char* ws;
  unsigned long long pad;
};

__global__ void __launch_bounds__(NTHR, 2) paged_attn_kernel(Params p) {
  extern __shared__ __attribute__((aligned(16))) char smem[];
  LAS char* lds = (LAS char*)smem;
  const int tid = threadIdx.x, lane = tid & 63, wave = rfl(tid >> 6);
  const int h = blockIdx.x % H, b = blockIdx.x / H;
  const int g = lane >> 4, c = lane & 15, r8 = lane & 7;
  const u64 t_entry = __builtin_amdgcn_s_memrealtime();

  i32x4 tb[B];
#pragma unroll
  for (int r = 0; r < B; ++r) tb[r] = *(const i32x4*)(p.bt + r * NB + lane * 4);
  const int lenr = p.lens[r8];
  u64 epoch = 0;
  if (wave == NSW) epoch = __hip_atomic_load((GAS u64*)(p.ws + WS_EPOCH + h * 128), __ATOMIC_RELAXED, __HIP_MEMORY_SCOPE_AGENT);

  unsigned voff[6];
#pragma unroll
  for (int i = 0; i < 6; ++i) {
    const int pp = i * 64 + lane, r_ = (pp * 2731) >> 16, ch = pp - 24 * r_;
    voff[i] = (unsigned)(r_ * ROWB + (ch ^ (r_ & 6)) * 16 - (i & 3) * 1024);
  }
  const unsigned short* qp = p.q + ((c & 7) * H + h) * D + 8 * g;
  if (tid == 0) *(LAS u32x2*)(lds + L_SYNC) = u32x2{0u, 0u};
  asm volatile("" : "+v"(voff[0]), "+v"(voff[1]), "+v"(voff[2]), "+v"(voff[3]), "+v"(voff[4]), "+v"(voff[5]), "+v"(qp));
  asm volatile("" : "+v"(tb[0]));

  int P = NB;
  {
    u64 mk[4];
#pragma unroll
    for (int j = 0; j < 4; ++j) {
      const int mn = min(min(min(tb[0][j], tb[1][j]), min(tb[2][j], tb[3][j])), min(min(tb[4][j], tb[5][j]), min(tb[6][j], tb[7][j])));
      const int mx = max(max(max(tb[0][j], tb[1][j]), max(tb[2][j], tb[3][j])), max(max(tb[4][j], tb[5][j]), max(tb[6][j], tb[7][j])));
      mk[j] = __ballot(mn != mx);
    }
#pragma unroll
    for (int j = 0; j < 4; ++j) if (mk[j]) P = min(P, 4 * (int)__builtin_ctzll(mk[j]) + j);
  }
  const int lenv = (lenr <= 0 || lenr > LK) ? LK : lenr;
  const int np = (lenv + BS - 1) / BS;
  const int n = max(0, np - P);
  int sumn = n, maxnp = np;
  sumn += dpp<DPP_X1>(sumn); maxnp = max(maxnp, dpp<DPP_X1>(maxnp));
  sumn += dpp<DPP_X2>(sumn); maxnp = max(maxnp, dpp<DPP_X2>(maxnp));
  sumn += dpp<DPP_HMIRROR>(sumn); maxnp = max(maxnp, dpp<DPP_HMIRROR>(maxnp));
  const int S = min(P, rfl(maxnp));
  const int T = S + rfl(sumn);
  const int quota = ((T * (r8 + 1)) >> 3) - ((T * r8) >> 3);
  const int key = n * 8 + r8;
  int ko[7];
  ko[0] = dpp<DPP_ROR + 1>(key); ko[1] = dpp<DPP_ROR + 2>(key); ko[2] = dpp<DPP_ROR + 3>(key); ko[3] = dpp<DPP_ROR + 4>(key);
  ko[4] = dpp<DPP_ROR + 5>(key); ko[5] = dpp<DPP_ROR + 6>(key); ko[6] = dpp<DPP_ROR + 7>(key);
  int rank = 0;
#pragma unroll
  for (int j = 0; j < 7; ++j) rank += ko[j] < key ? 1 : 0;
  int share, ntake;
  {
    const int qmin = T >> 3, room = max(1, qmin - (qmin >> 2));
    ntake = 1;
#pragma unroll
    for (int j = 1; j < 8; ++j) ntake += j * room < S ? 1 : 0;
    share = S > 0 ? (int)((float)S * __builtin_amdgcn_rcpf((float)ntake)) + 1 : 0;
  }
  const int own = min(n, rank < ntake ? max(quota - share, 0) : quota);
  const int cap = quota - own, ovf = n - own;
  int capPre = 0, ovfPre = 0;
  {
    int co[7], oo[7];
    co[0] = dpp<DPP_ROR + 1>(cap); co[1] = dpp<DPP_ROR + 2>(cap); co[2] = dpp<DPP_ROR + 3>(cap); co[3] = dpp<DPP_ROR + 4>(cap);
    co[4] = dpp<DPP_ROR + 5>(cap); co[5] = dpp<DPP_ROR + 6>(cap); co[6] = dpp<DPP_ROR + 7>(cap);
    oo[0] = dpp<DPP_ROR + 1>(ovf); oo[1] = dpp<DPP_ROR + 2>(ovf); oo[2] = dpp<DPP_ROR + 3>(ovf); oo[3] = dpp<DPP_ROR + 4>(ovf);
    oo[4] = dpp<DPP_ROR + 5>(ovf); oo[5] = dpp<DPP_ROR + 6>(ovf); oo[6] = dpp<DPP_ROR + 7>(ovf);
#pragma unroll
    for (int j = 0; j < 7; ++j) { capPre += ko[j] < key ? co[j] : 0; ovfPre += (ko[j] & 7) < r8 ? oo[j] : 0; }
  }
  const int cap_b = rdl(cap, b), capPre_b = rdl(capPre, b), q_b = rdl(quota, b), ovf_b = rdl(ovf, b), ovfPre_b = rdl(ovfPre, b);

  if (wave < NSW) {
    int ent;
    {
      const int it = wave + NSW * lane;
      const int f = capPre_b + it - S;
      int dn = 0, base = 0;
#pragma unroll
      for (int r = 1; r < B; ++r) { const int s = rdl(ovfPre, r); if (f >= s) { dn = r; base = s; } }
      const bool fo = it < cap_b, sh = f < 0;
      const int row = fo ? (sh ? 0 : dn) : b, tag = fo ? (sh ? 8 : dn) : b;
      int col = fo ? (sh ? f + S : P + f - base) : P + ovf_b + it - cap_b;
      col = min(max(col, 0), NB - 1);
      const int phys = p.bt[row * NB + col];
      ent = (int)(((unsigned)phys << 12) | (unsigned)(tag << 8) | (unsigned)col);
    }
    const int nw = q_b > wave ? (q_b - wave + NSW - 1) / NSW : 0;
    const int i1 = cap_b > wave ? (cap_b - wave + NSW - 1) / NSW : 0;
    const int ntiles = 2 * nw;
    asm volatile("" : "+v"(ent));
    u32x4 qr[6];
    asm volatile(
        "global_load_dwordx4 %0, %6, off\n\t"
        "global_load_dwordx4 %1, %6, off offset:64\n\t"
        "global_load_dwordx4 %2, %6, off offset:128\n\t"
        "global_load_dwordx4 %3, %6, off offset:192\n\t"
        "global_load_dwordx4 %4, %6, off offset:256\n\t"
        "global_load_dwordx4 %5, %6, off offset:320"
        : "=&v"(qr[0]), "=&v"(qr[1]), "=&v"(qr[2]), "=&v"(qr[3]), "=&v"(qr[4]), "=&v"(qr[5])
        : "v"(qp)
        : "memory");

    const unsigned ring = (unsigned)(size_t)(lds + L_RING) + wave * R * TILE;
    const char* kh = p.k + h * (D * 2);
    const char* vh = p.v + h * (D * 2);
    int slot_issue = 0;
    int issued = 0;
    const unsigned t_p0 = now32();
    unsigned step_q, due_q;
    {
      constexpr unsigned kap = KAPPA, lead = LEAD, pc0 = PC0;
      const int span = (int)((unsigned)t_entry + pc0 + (unsigned)(T * kap) / 1000u - t_p0);
      step_q = span > 0 && ntiles > 0 ? (unsigned)((float)span * 256.f * __builtin_amdgcn_rcpf((float)ntiles)) : 0u;
      step_q = (unsigned)rfl((int)step_q);
      due_q = 0u - lead * step_q;
    }
    auto issue = [&]() {
      const unsigned e = (unsigned)rdl(ent, issued >> 1);
      const char* base = ((issued & 1) ? vh : kh) + (size_t)(e >> 12) * PAGEB;
      due_q += step_q;
      dma_tile(base, ring + slot_issue, voff[0], voff[1], voff[2], voff[3], voff[4], voff[5]);
      ++issued;
      slot_issue += TILE; if (slot_issue == R * TILE) slot_issue = 0;
    };
    auto pump = [&](int done, int need) {
      int lim = min(ntiles, done + R);
      while (issued < lim) {
        if ((int)(((now32() - t_p0) << 8) - due_q) < 0) {
          if (issued > need) break;
          __builtin_amdgcn_s_sleep(1);
          continue;
        }
        issue();
      }
    };
    pump(0, 0);
    __builtin_amdgcn_s_barrier();
    wait_tiles(issued);
    asm volatile("" : "+v"(qr[0]), "+v"(qr[1]), "+v"(qr[2]), "+v"(qr[3]), "+v"(qr[4]), "+v"(qr[5]));
    const int lenc = c >= 8 ? 0 : lenv;
    bf16x8 qf[6];
    {
      const bool z = c >= 8 || lenr <= 0;
#pragma unroll
      for (int s = 0; s < 6; ++s) qf[s] = __builtin_bit_cast(bf16x8, u32x4{z ? 0u : qr[s][0], z ? 0u : qr[s][1], z ? 0u : qr[s][2], z ? 0u : qr[s][3]});
    }

    unsigned ka[2], va[4];
#pragma unroll
    for (int e = 0; e < 2; ++e) ka[e] = ring + (unsigned)(16 * (24 * c + ((4 * e + g) ^ (c & 6))));
    {
      const int row = 4 * g + (c >> 2), pq = lane & 3, x = (row >> 1) & 3;
#pragma unroll
      for (int nl = 0; nl < 4; ++nl) va[nl] = ring + (unsigned)(16 * 24 * row + 32 * (nl ^ x) + 16 * (pq >> 1) + 8 * (pq & 1));
    }

    float m = -INFINITY, l = 0.f;
    f32x4 acc[12];
#pragma unroll
    for (int i = 0; i < 12; ++i) acc[i] = f32x4{0.f, 0.f, 0.f, 0.f};

    auto dump1 = [&]() {
      const float ls = xrow_sum(l);
      LAS char* dst = lds + L_DUMP1 + wave * DUMP1_W;
      if (c < 8) {
#pragma unroll
        for (int i = 0; i < 12; ++i) *(LAS f32x4*)(dst + c * QSTR + i * 64 + g * 16) = acc[i];
        if (g == 0) *(LAS f32x2*)(lds + L_STAT1 + wave * 64 + c * 8) = f32x2{m, ls};
      }
      asm volatile("s_waitcnt lgkmcnt(0)" ::: "memory");
      if (lane == 0) __hip_atomic_fetch_add((LAS unsigned*)(lds + L_SYNC), 1u, __ATOMIC_RELAXED, __HIP_MEMORY_SCOPE_WORKGROUP);
      const bool keep = c == b;
      m = keep ? m : -INFINITY; l = keep ? l : 0.f;
#pragma unroll
      for (int i = 0; i < 12; ++i) acc[i] = sel0(keep, acc[i]);
    };

    int slot = 0;
#pragma unroll 1
    for (int i = 0; i < nw; ++i) {
      if (i == i1) { dump1();
      }
      const unsigned e = (unsigned)rdl(ent, i);
      const int col = e & 255, tag = (e >> 8) & 15;
      wait_tiles(issued - 2 * i - 1);
      bf16x8 kf[6];
#pragma unroll
      for (int s = 0; s < 6; ++s) kf[s] = *(const LAS bf16x8*)(size_t)(ka[s & 1] + slot + 128 * (s >> 1));
      __builtin_amdgcn_sched_barrier(0);
      f32x4 s0 = {0.f, 0.f, 0.f, 0.f}, s1 = {0.f, 0.f, 0.f, 0.f};
#pragma unroll
      for (int s = 0; s < 6; s += 2) {
        s0 = __builtin_amdgcn_mfma_f32_16x16x32_bf16(kf[s], qf[s], s0, 0, 0, 0);
        s1 = __builtin_amdgcn_mfma_f32_16x16x32_bf16(kf[s + 1], qf[s + 1], s1, 0, 0, 0);
      }
      slot += TILE; if (slot == R * TILE) slot = 0;
      pump(2 * i + 1, 2 * i + 1);
      const int rem = ((tag == 8 || tag == c) ? lenc : 0) - col * BS - 4 * g;
      float x[4];
#pragma unroll
      for (int j = 0; j < 4; ++j) x[j] = j < rem ? (s0[j] + s1[j]) * C2 : -INFINITY;
      const float pm = xrow_max(fmaxf(fmaxf(x[0], x[1]), fmaxf(x[2], x[3])));
      const float mn = fmaxf(m, pm);
      if (__any(mn > m)) {
        const float alpha = mn > m ? __builtin_amdgcn_exp2f(m - mn) : 1.f;
        l *= alpha;
#pragma unroll
        for (int ii = 0; ii < 12; ++ii) acc[ii] *= alpha;
        m = mn;
      }
      const float msub = m == -INFINITY ? 0.f : m;
      float pr[4];
#pragma unroll
      for (int j = 0; j < 4; ++j) pr[j] = __builtin_amdgcn_exp2f(x[j] - msub);
      l += (pr[0] + pr[1]) + (pr[2] + pr[3]);
      const s16x4 pf = __builtin_bit_cast(s16x4, u32x2{pack_bf16(pr[0], pr[1]), pack_bf16(pr[2], pr[3])});
      wait_tiles(issued - 2 * i - 2);
      s16x4 vf[12];
#pragma unroll
      for (int nn = 0; nn < 12; ++nn)
        vf[nn] = __builtin_amdgcn_ds_read_tr16_b64_v4i16((LAS s16x4*)(size_t)(va[nn & 3] + slot + 128 * (nn >> 2)));
      __builtin_amdgcn_sched_barrier(0);
#pragma unroll
      for (int nn = 0; nn < 12; ++nn) acc[nn] = __builtin_amdgcn_mfma_f32_16x16x16bf16_1k(vf[nn], pf, acc[nn], 0, 0, 0);
      slot += TILE; if (slot == R * TILE) slot = 0;
      pump(2 * i + 2, 2 * i + 2);
    }
    if (i1 >= nw) dump1();
    {
      const float ls = xrow_sum(l);
      LAS char* dst = lds + L_DUMP2 + wave * QSTR;
      if (c == b) {
#pragma unroll
        for (int i = 0; i < 12; ++i) *(LAS f32x4*)(dst + i * 64 + g * 16) = acc[i];
        if (g == 0) *(LAS f32x2*)(dst + 768) = f32x2{m, ls};
      }
      asm volatile("s_waitcnt lgkmcnt(0)" ::: "memory");
      if (lane == 0) __hip_atomic_fetch_add((LAS unsigned*)(lds + L_SYNC + 4), 1u, __ATOMIC_RELAXED, __HIP_MEMORY_SCOPE_WORKGROUP);
    }
  } else {
    asm volatile("s_waitcnt lgkmcnt(0)" ::: "memory");
    __builtin_amdgcn_s_barrier();
    const u64 tag = epoch + 1;
    volatile LAS unsigned* sync = (volatile LAS unsigned*)(lds + L_SYNC);
    char* rec0 = p.ws + WS_REC + (size_t)(h * B) * REC_B;
    unsigned colmask;
    bool need;
    {
      const int lo_c = S + ovfPre, hi_c = lo_c + ovf;
      const bool mine = cap_b > 0 && (capPre_b < S || max(capPre_b, lo_c) < min(capPre_b + cap_b, hi_c));
      colmask = (unsigned)__ballot(mine && r8 != b) & 255u;
      const int lo_b = S + ovfPre_b, hi_b = lo_b + ovf_b;
      need = r8 != b && cap > 0 && (capPre < S || max(capPre, lo_b) < min(capPre + cap, hi_b));
    }
    while (sync[0] < NSW) __builtin_amdgcn_s_sleep(4);
    asm volatile("" ::: "memory");
    if (colmask) {
      const int w4 = (lane >> 3) & 3;
      const f32x2 st = *(const LAS f32x2*)(lds + L_STAT1 + w4 * 64 + r8 * 8);
      float M = st[0];
      M = fmaxf(M, __shfl_xor(M, 8)); M = fmaxf(M, __shfl_xor(M, 16));
      const float f = st[1] > 0.f ? __builtin_amdgcn_exp2f(st[0] - M) : 0.f;
      float L = f * st[1];
      L += __shfl_xor(L, 8); L += __shfl_xor(L, 16);
      const auto rs = __builtin_amdgcn_make_buffer_rsrc(uni_ptr(rec0 + (size_t)b * REC_B), 0, REC_B, 0x00020000);
#pragma unroll
      for (int i = 0; i < 3; ++i) {
        const int pp = i * 64 + lane, cq = (pp * 2731) >> 16, kk = pp - 24 * cq;
        f32x4 a0 = {0.f, 0.f, 0.f, 0.f}, a1 = {0.f, 0.f, 0.f, 0.f};
#pragma unroll
        for (int w = 0; w < NSW; ++w) {
          const float fw = __shfl(f, w * 8 + cq);
          const LAS char* src = lds + L_DUMP1 + w * DUMP1_W + cq * QSTR + kk * 32;
          a0 += fw * sel0(fw > 0.f, *(const LAS f32x4*)src);
          a1 += fw * sel0(fw > 0.f, *(const LAS f32x4*)(src + 16));
        }
        const u32x4 o = {pack_bf16(a0[0], a0[1]), pack_bf16(a0[2], a0[3]), pack_bf16(a1[0], a1[1]), pack_bf16(a1[2], a1[3])};
        if ((colmask >> cq) & 1) __builtin_amdgcn_raw_buffer_store_b128(o, rs, 128 + pp * 16, 0, 16);
      }
      const int sl = (lane & 3) * 2;
      const f32x4 hd = {__shfl(M, sl), __shfl(L, sl), __shfl(M, sl + 1), __shfl(L, sl + 1)};
      if (lane < 4) __builtin_amdgcn_raw_buffer_store_b128(__builtin_bit_cast(u32x4, hd), rs, lane * 16, 0, 16);
    }
    asm volatile("s_waitcnt vmcnt(0)" ::: "memory");
    if (lane == 0) __hip_atomic_store((GAS u64*)(p.ws + WS_FLAG) + h * B + b, tag, __ATOMIC_RELAXED, __HIP_MEMORY_SCOPE_AGENT);

    {
      GAS u64* fl = (GAS u64*)(p.ws + WS_FLAG) + h * B + r8;
      const bool wt = need || b == 0;
      for (unsigned spins = 0; spins < (1u << 22); ++spins) {
        const u64 x = __hip_atomic_load(fl, __ATOMIC_RELAXED, __HIP_MEMORY_SCOPE_AGENT);
        if (__all(!wt || x == tag)) break;
        __builtin_amdgcn_s_sleep(16);
      }
      asm volatile("" ::: "memory");
    }
    if (b == 0 && lane == 0) __hip_atomic_store((GAS u64*)(p.ws + WS_EPOCH + h * 128), tag, __ATOMIC_RELAXED, __HIP_MEMORY_SCOPE_AGENT);
    f32x2 hd = {-INFINITY, 0.f};
    if (need) hd = __builtin_bit_cast(f32x2, __hip_atomic_load((GAS u64*)(rec0 + (size_t)r8 * REC_B + b * 8), __ATOMIC_RELAXED, __HIP_MEMORY_SCOPE_AGENT));
    float MF = hd[1] > 0.f ? hd[0] : -INFINITY;
    MF = fmaxf(MF, __shfl_xor(MF, 1)); MF = fmaxf(MF, __shfl_xor(MF, 2)); MF = fmaxf(MF, __shfl_xor(MF, 4));
    const float fr = hd[1] > 0.f ? __builtin_amdgcn_exp2f(hd[0] - MF) : 0.f;
    float LF = fr * hd[1];
    LF += __shfl_xor(LF, 1); LF += __shfl_xor(LF, 2); LF += __shfl_xor(LF, 4);
    f32x4 aF = {0.f, 0.f, 0.f, 0.f};
    {
      const auto rs = __builtin_amdgcn_make_buffer_rsrc(uni_ptr(rec0), 0, B * REC_B, 0x00020000);
      const int lo = min(lane, 47) * 8;
      u32x2 x[B];
#pragma unroll
      for (int r = 0; r < B; ++r) x[r] = __builtin_amdgcn_raw_buffer_load_b64(rs, r * REC_B + 128 + b * QREC + lo, 0, 16);
#pragma unroll
      for (int r = 0; r < B; ++r) {
        const float fw = __shfl(fr, r);
        const f32x4 y = {__uint_as_float(x[r][0] << 16), __uint_as_float(x[r][0] & 0xffff0000u), __uint_as_float(x[r][1] << 16), __uint_as_float(x[r][1] & 0xffff0000u)};
        aF += fw * sel0(fw > 0.f, y);
      }
    }
    while (sync[1] < NSW) __builtin_amdgcn_s_sleep(1);
    asm volatile("" ::: "memory");
    {
      f32x2 st[NSW];
      float M = MF;
#pragma unroll
      for (int w = 0; w < NSW; ++w) { st[w] = *(const LAS f32x2*)(lds + L_DUMP2 + w * QSTR + 768); if (st[w][1] > 0.f) M = fmaxf(M, st[w][0]); }
      const float f0 = LF > 0.f ? __builtin_amdgcn_exp2f(MF - M) : 0.f;
      float L = f0 * LF;
      f32x4 a = f0 * aF;
      const int lo = min(lane, 47) * 16;
#pragma unroll
      for (int w = 0; w < NSW; ++w) {
        const float fw = st[w][1] > 0.f ? __builtin_amdgcn_exp2f(st[w][0] - M) : 0.f;
        const f32x4 x = *(const LAS f32x4*)(lds + L_DUMP2 + w * QSTR + lo);
        L += fw * st[w][1];
        a += fw * sel0(fw > 0.f, x);
      }
      const float inv = 1.f / L;
      if (lane < 48) {
        uint2 o = make_uint2(pack_bf16(a[0] * inv, a[1] * inv), pack_bf16(a[2] * inv, a[3] * inv));
        *(uint2*)(p.out + (b * H + h) * D + lane * 4) = o;
      }
    }
  }
}

}

extern "C" void kernel_launch(void* const* d_in, const int* in_sizes, int n_in, void* d_out, int out_size, void* d_ws, size_t ws_size,
                              hipStream_t stream) {
  (void)hipFuncSetAttribute((const void*)paged_attn_kernel, hipFuncAttributeMaxDynamicSharedMemorySize, LDS_BYTES);
  Params p;
  p.bt = (const int*)d_in[3];
  p.lens = (const int*)d_in[4];
  p.q = (const unsigned short*)d_in[0];
  p.k = (const char*)d_in[1];
  p.v = (const char*)d_in[2];
  p.out = (unsigned short*)d_out;
  p.ws = (char*)d_ws;
  p.pad = 0;
  hipLaunchKernelGGL(paged_attn_kernel, dim3(H * B), dim3(NTHR), LDS_BYTES, stream, p);
}
```

```cpp
#include <hip/hip_runtime.h>
#include <hip/hip_bf16.h>
#include <stdint.h>

namespace {

constexpr int B = 8, H = 32, D = 192, BS = 16, NB = 256, LK = NB * BS;
constexpr int ROWB = H * D * 2;
constexpr int PAGEB = BS * ROWB;
constexpr int TILE = BS * D * 2;
#define RING 3
constexpr int R = RING;
constexpr int NSW = 4;
constexpr int NTHR = (NSW + 3) * 64;
#define KAPPA 5100
#define PC0 166
#define LEAD 1
#define PERIOD 270
#define FSTOP 230
#define NSAMP 64
#define TBASE1 500
#define TD1 880
#define KMIN 4900.f
#define SOFF 80
#define TLATE 2500.f
#define XLATE 0.04f
#define LATEW 1.f
#define TAPER 15
#define FSLOPE 0.02f
#define SLEW 0.004f
#define KPU 0.16f
#define TFAST 1700.f
#define TLATE0 2499.f
#define RSV 16
#define POLLP 100
#define KMAX 5600.f
#define KMAX2 6100.f
#define LBAD 99.f
#define TBAD 1300.f
#define LMBAD 3.0f
#define GX0 0.3f
#define TGUARD 1500
#define K1 0.8f
#define X0 0.088f
#define X1MIN 0.f
#define X1MAX 0.f
#define TSW 150
#define REM_MIN 3
#define KP 0.16f
#define KI 0.016f
#define KIUP 0.016f
#define FL 1.2f
#define FX 2.2f
#define GX 0.3f
#define RAS 3
#define RAN 3
#define RBS 2
#define RBN 2
#define LA0 2.0f
#define INI 3
#define PUMPK 1
#define RK 2
#define DKPH 0.3f
#define XKQ 16
#define KPF 8
#define VPF 8
#define VPFT 250
#define DKW 6.f
#define KVPH 20
#define CPOLL 2
#define KV1 5
#define CPF 148
#define RSW 0
#define TFIRST 350

constexpr int L_RING = 0;
constexpr int QSTR = 784;
constexpr int L_DUMP1 = NSW * R * TILE;
constexpr int DUMP1_W = 8 * QSTR;
constexpr int L_STAT1 = L_DUMP1 + NSW * DUMP1_W;
constexpr int L_DUMP2 = L_STAT1 + NSW * 64;
constexpr int L_SYNC = L_DUMP2 + NSW * QSTR;
constexpr int L_TAG = L_SYNC + 40;
constexpr int L_PROG = L_SYNC + 48;
constexpr int L_PACE = L_SYNC + 64;
constexpr int L_TAB = L_SYNC + 96;
constexpr int L_PST = L_TAB + B * NB * 4;
constexpr int PSTB = 288;
constexpr int L_JUNK = L_PST + NSW * KVPH * PSTB;
constexpr int LDS_BYTES = L_JUNK + 3 * 256;
static_assert(LDS_BYTES <= 160 * 1024 && KV1 <= KVPH && KV1 >= 2, "LDS");

#define FLAG_STRIDE 256
constexpr size_t WS_EPOCH = 0;
constexpr size_t WS_LINE = WS_EPOCH + H * 256;
constexpr size_t WS_PROG = WS_LINE + H * 256;
constexpr size_t WS_FLAG = WS_PROG + 64 * 256;
constexpr size_t WS_REC = WS_FLAG + H * B * 256;
#define QRS 512
constexpr int QREC = QRS;
constexpr int QOFF = QRS - 512;
constexpr int REC_B = 8 * QREC;
constexpr size_t WS_END = WS_REC + (size_t)H * B * REC_B;

constexpr float C2 = 0.07216878364870322f * 1.4426950408889634f;

#define LAS __attribute__((address_space(3)))
#define GAS __attribute__((address_space(1)))
typedef __attribute__((ext_vector_type(4))) unsigned u32x4;
typedef __attribute__((ext_vector_type(2))) unsigned u32x2;
typedef __attribute__((ext_vector_type(4))) int i32x4;
typedef __attribute__((ext_vector_type(4))) float f32x4;
typedef __attribute__((ext_vector_type(2))) float f32x2;
typedef __attribute__((ext_vector_type(8))) __bf16 bf16x8;
typedef __attribute__((ext_vector_type(4))) short s16x4;
typedef __attribute__((ext_vector_type(2))) __bf16 bf16x2;
typedef unsigned long long u64;

__device__ __forceinline__ int rfl(int x) { return __builtin_amdgcn_readfirstlane(x); }
__device__ __forceinline__ int rdl(int x, int l) { return __builtin_amdgcn_readlane(x, l); }
template <int CTRL> __device__ __forceinline__ int dpp(int x) { return __builtin_amdgcn_mov_dpp(x, CTRL, 0xf, 0xf, true); }
constexpr int DPP_X1 = 0xB1, DPP_X2 = 0x4E, DPP_HMIRROR = 0x141, DPP_ROR = 0x120, DPP_SHR = 0x110;
__device__ __forceinline__ float xrow_max(float x) {
  auto s = __builtin_amdgcn_permlane16_swap(__float_as_uint(x), __float_as_uint(x), false, false);
  x = fmaxf(__uint_as_float(s[0]), __uint_as_float(s[1]));
  auto t = __builtin_amdgcn_permlane32_swap(__float_as_uint(x), __float_as_uint(x), false, false);
  return fmaxf(__uint_as_float(t[0]), __uint_as_float(t[1]));
}
__device__ __forceinline__ float xrow_sum(float x) {
  auto s = __builtin_amdgcn_permlane16_swap(__float_as_uint(x), __float_as_uint(x), false, false);
  x = __uint_as_float(s[0]) + __uint_as_float(s[1]);
  auto t = __builtin_amdgcn_permlane32_swap(__float_as_uint(x), __float_as_uint(x), false, false);
  return __uint_as_float(t[0]) + __uint_as_float(t[1]);
}
template <int CTRL> __device__ __forceinline__ float dppf(float x) { return __int_as_float(dpp<CTRL>(__float_as_int(x))); }
__device__ __forceinline__ float wave_sum(float x) {
  x += dppf<DPP_X1>(x); x += dppf<DPP_X2>(x); x += dppf<DPP_HMIRROR>(x); x += dppf<DPP_ROR + 8>(x);
  return xrow_sum(x);
}
__device__ __forceinline__ f32x4 sel0(bool c, f32x4 x) { return f32x4{c ? x[0] : 0.f, c ? x[1] : 0.f, c ? x[2] : 0.f, c ? x[3] : 0.f}; }
template <typename T> __device__ __forceinline__ T* uni_ptr(T* p) {
  const unsigned long long u = (unsigned long long)p;
  const unsigned lo = __builtin_amdgcn_readfirstlane((unsigned)u), hi = __builtin_amdgcn_readfirstlane((unsigned)(u >> 32));
  return (T*)(((unsigned long long)hi << 32) | lo);
}
__device__ __forceinline__ unsigned pack_bf16(float lo, float hi) {
  bf16x2 v = {(__bf16)lo, (__bf16)hi};
  return __builtin_bit_cast(unsigned, v);
}
__device__ __forceinline__ unsigned now32() { return (unsigned)__builtin_amdgcn_s_memrealtime(); }

__device__ __forceinline__ void dma_tile(const void* sbase, unsigned ldsaddr, unsigned v0, unsigned v1, unsigned v2, unsigned v3,
                                         unsigned v4, unsigned v5) {
  unsigned l2 = ldsaddr + 4096;
  asm volatile(
      "s_waitcnt lgkmcnt(0)\n\t"
      "s_mov_b32 m0, %7\n\t"
      "s_nop 0\n\t"
      "global_load_lds_dwordx4 %0, %6 nt\n\t"
      "global_load_lds_dwordx4 %1, %6 offset:1024 nt\n\t"
      "global_load_lds_dwordx4 %2, %6 offset:2048 nt\n\t"
      "global_load_lds_dwordx4 %3, %6 offset:3072 nt\n\t"
      "s_mov_b32 m0, %8\n\t"
      "s_nop 0\n\t"
      "global_load_lds_dwordx4 %4, %6 nt\n\t"
      "global_load_lds_dwordx4 %5, %6 offset:1024 nt"
      :
      : "v"(v0), "v"(v1), "v"(v2), "v"(v3), "v"(v4), "v"(v5), "s"(sbase), "s"(ldsaddr), "s"(l2)
      : "memory");
}
__device__ __forceinline__ void wait_tiles(int k) {
  if (k >= 5) asm volatile("s_waitcnt vmcnt(30)" ::: "memory");
  else if (k == 4) asm volatile("s_waitcnt vmcnt(24)" ::: "memory");
  else if (k == 3) asm volatile("s_waitcnt vmcnt(18)" ::: "memory");
  else if (k == 2) asm volatile("s_waitcnt vmcnt(12)" ::: "memory");
  else if (k == 1) asm volatile("s_waitcnt vmcnt(6)" ::: "memory");
  else asm volatile("s_waitcnt vmcnt(0)" ::: "memory");
}
static_assert(R >= 3 && R <= 5, "wait_tiles, first tiles");

struct Params {
  const int* bt;
  const int* lens;
  const unsigned short* q;
  const char* k;
  const char* v;
  unsigned short* out;
  char* ws;
  unsigned long long kvb;
  unsigned long long pad;
};

__global__ void __launch_bounds__(NTHR, 2) paged_attn_kernel(Params p) {
  extern __shared__ __attribute__((aligned(16))) char smem[];
  LAS char* lds = (LAS char*)smem;
  const int tid = threadIdx.x, lane = tid & 63, wave = rfl(tid >> 6);
  {
    unsigned long long pc0;
    asm volatile("s_getpc_b64 %0" : "=s"(pc0));
    const int ln = (wave - NSW) * 64 + lane;
    if (wave >= NSW && ln < CPF)
      asm volatile("s_mov_b32 m0, %2\n\ts_nop 0\n\tglobal_load_lds_dword %0, %1"
                   : : "v"(ln * 128), "s"(pc0 & ~127ull), "s"((unsigned)(size_t)lds + L_JUNK + (wave - NSW) * 256) : "memory");
  }
  const int h = blockIdx.x / B, b = (blockIdx.x + blockIdx.x / B) % B;
  const int g = lane >> 4, c = lane & 15, r8 = lane & 7;
  const u64 t_entry = __builtin_amdgcn_s_memrealtime();

  if (wave > NSW) {
    if (wave == NSW + 2 && lane < KPF) {
      const unsigned stride = (unsigned)((p.kvb / (32 * KPF)) & ~63ull);
      asm volatile("s_mov_b32 m0, %2\n\ts_nop 0\n\tglobal_load_lds_dword %0, %1"
                   : : "v"((((blockIdx.x >> 3) & 31) * KPF + lane) * stride), "s"(p.k), "s"((unsigned)(size_t)lds + L_JUNK + 512) : "memory");
    }
    if (wave == NSW + 1 && lane < 3) {
      const char* a = lane == 0 ? p.ws + WS_REC + (size_t)(h * B) * REC_B : lane == 1 ? p.ws + WS_FLAG + (h * B) * FLAG_STRIDE : (const char*)(p.out + (b * H + h) * D);
      asm volatile("s_mov_b32 m0, %1\n\ts_nop 0\n\tglobal_load_lds_dword %0, off" : : "v"(a), "s"((unsigned)(size_t)lds + L_JUNK + 256) : "memory");
    }
    __builtin_amdgcn_s_barrier();
    volatile LAS unsigned* sync = (volatile LAS unsigned*)(lds + L_SYNC);
    const u32x2 tg = *(volatile LAS u32x2*)(lds + L_TAG);
    const u64 tag = ((u64)(unsigned)rfl((int)tg[1]) << 32) | (unsigned)rfl((int)tg[0]);
    const u64 tagw = tag << 8;
    char* rec0 = p.ws + WS_REC + (size_t)(h * B) * REC_B;
    if (wave == NSW + 1) {
      unsigned done = 1u << b;
      const auto rs = __builtin_amdgcn_make_buffer_rsrc(uni_ptr(rec0 + (size_t)b * REC_B), 0, REC_B, 0x00020000);
      asm volatile("s_waitcnt vmcnt(0)" ::: "memory");
      while (done != 255u) {
        unsigned ready = (unsigned)__ballot(sync[r8] >= NSW) & 255u & ~done;
        asm volatile("" ::: "memory");
        if (!ready) { __builtin_amdgcn_s_sleep(2); continue; }
        done |= ready;
        while (ready) {
          const int cq = __builtin_ctz(ready);
          ready &= ready - 1;
          f32x2 st[NSW];
          float M = -INFINITY;
#pragma unroll
          for (int w = 0; w < NSW; ++w) { st[w] = *(const LAS f32x2*)(lds + L_STAT1 + w * 64 + cq * 8); if (st[w][1] > 0.f) M = fmaxf(M, st[w][0]); }
          float L = 0.f;
          f32x4 a = {0.f, 0.f, 0.f, 0.f};
#pragma unroll
          for (int w = 0; w < NSW; ++w) {
            const float fw = st[w][1] > 0.f ? __builtin_amdgcn_exp2f(st[w][0] - M) : 0.f;
            const f32x4 x = *(const LAS f32x4*)(lds + L_DUMP1 + w * DUMP1_W + cq * QSTR + min(lane, 47) * 16);
            L += fw * st[w][1];
            a += fw * sel0(fw > 0.f, x);
          }
          const u32x2 o = lane < 48 ? u32x2{pack_bf16(a[0], a[1]), pack_bf16(a[2], a[3])} : u32x2{__float_as_uint(M), __float_as_uint(L)};
          __builtin_amdgcn_raw_buffer_store_b64(o, rs, cq * QREC + QOFF + lane * 8, 0, 16);
        }
        asm volatile("s_waitcnt vmcnt(0)" ::: "memory");
        {
          const u64 fw = tagw | (done & ~(1u << b));
          if (lane < 8) __builtin_amdgcn_raw_buffer_store_b128(u32x4{lane == 0 ? (unsigned)fw : 0u, lane == 0 ? (unsigned)(fw >> 32) : 0u, 0u, 0u},
                                                               __builtin_amdgcn_make_buffer_rsrc(uni_ptr(p.ws + WS_FLAG + (h * B + b) * FLAG_STRIDE), 0, 128, 0x00020000), lane * 16, 0, 16);
        }
      }
    } else {
      float MF = -INFINITY, LF = 0.f;
      f32x4 aF = {0.f, 0.f, 0.f, 0.f};
      asm volatile("s_waitcnt vmcnt(0)" ::: "memory");
      while (sync[b] < NSW) __builtin_amdgcn_s_sleep(4);
      for (unsigned spins = 0; spins < (1u << 22); ++spins) {
        const u64 x = __hip_atomic_load((GAS u64*)(p.ws + WS_FLAG + (h * B + r8) * FLAG_STRIDE), __ATOMIC_RELAXED, __HIP_MEMORY_SCOPE_AGENT);
        const bool got = __all(r8 == b || ((x >> 8) == (tagw >> 8) && ((x >> b) & 1)));
        if (got) break;
        __builtin_amdgcn_s_sleep(CPOLL);
      }
      asm volatile("" ::: "memory");
      if (b == 0 && lane == 0) __hip_atomic_store((GAS u64*)(p.ws + WS_EPOCH + h * 256), tag, __ATOMIC_RELAXED, __HIP_MEMORY_SCOPE_AGENT);
      {
        const auto rs = __builtin_amdgcn_make_buffer_rsrc(uni_ptr(rec0), 0, B * REC_B, 0x00020000);
        const int lo = min(lane, 48) * 8;
        u32x2 x[B];
#pragma unroll
        for (int r = 0; r < B; ++r) x[r] = __builtin_amdgcn_raw_buffer_load_b64(rs, r * REC_B + b * QREC + QOFF + lo, 0, 16);
        float Mr[B], Lr[B];
#pragma unroll
        for (int r = 0; r < B; ++r) {
          Mr[r] = __int_as_float(rdl((int)x[r][0], 48));
          Lr[r] = r == b ? 0.f : __int_as_float(rdl((int)x[r][1], 48));
          if (Lr[r] > 0.f) MF = fmaxf(MF, Mr[r]);
        }
#pragma unroll
        for (int r = 0; r < B; ++r) {
          const float fw = Lr[r] > 0.f ? __builtin_amdgcn_exp2f(Mr[r] - MF) : 0.f;
          const f32x4 y = {__uint_as_float(x[r][0] << 16), __uint_as_float(x[r][0] & 0xffff0000u), __uint_as_float(x[r][1] << 16), __uint_as_float(x[r][1] & 0xffff0000u)};
          LF += fw * Lr[r];
          aF += fw * sel0(fw > 0.f, y);
        }
      }
      while (sync[8] < NSW) __builtin_amdgcn_s_sleep(1);
      asm volatile("" ::: "memory");
      f32x2 st[NSW];
      float M = MF;
#pragma unroll
      for (int w = 0; w < NSW; ++w) { st[w] = *(const LAS f32x2*)(lds + L_DUMP2 + w * QSTR + 768); if (st[w][1] > 0.f) M = fmaxf(M, st[w][0]); }
      const float f0 = LF > 0.f ? __builtin_amdgcn_exp2f(MF - M) : 0.f;
      float L = f0 * LF;
      f32x4 a = f0 * aF;
      const int lo = min(lane, 47) * 16;
#pragma unroll
      for (int w = 0; w < NSW; ++w) {
        const float fw = st[w][1] > 0.f ? __builtin_amdgcn_exp2f(st[w][0] - M) : 0.f;
        const f32x4 x = *(const LAS f32x4*)(lds + L_DUMP2 + w * QSTR + lo);
        L += fw * st[w][1];
        a += fw * sel0(fw > 0.f, x);
      }
      const float inv = 1.f / L;
      if (lane < 48) {
        uint2 o = make_uint2(pack_bf16(a[0] * inv, a[1] * inv), pack_bf16(a[2] * inv, a[3] * inv));
        *(uint2*)(p.out + (b * H + h) * D + lane * 4) = o;
      }
    }
    return;
  }

  u64 epoch = 0;
  if (wave == NSW) epoch = __hip_atomic_load((GAS u64*)(p.ws + WS_EPOCH + (blockIdx.x == 0 ? lane & 31 : h) * 256), __ATOMIC_RELAXED, __HIP_MEMORY_SCOPE_AGENT);
  i32x4 tb[B];
  int lenr;
  u32x4 qr[6];
  {
    const unsigned short* qp = p.q + ((c & 7) * H + h) * D + 8 * g;
    asm volatile(
        "global_load_dwordx4 %0, %15, %17\n\t"
        "global_load_dwordx4 %1, %15, %17 offset:1024\n\t"
        "global_load_dwordx4 %2, %15, %17 offset:2048\n\t"
        "global_load_dwordx4 %3, %15, %17 offset:3072\n\t"
        "global_load_dwordx4 %4, %16, %17\n\t"
        "global_load_dwordx4 %5, %16, %17 offset:1024\n\t"
        "global_load_dwordx4 %6, %16, %17 offset:2048\n\t"
        "global_load_dwordx4 %7, %16, %17 offset:3072\n\t"
        "global_load_dword %8, %18, %19\n\t"
        "global_load_dwordx4 %9, %20, off\n\t"
        "global_load_dwordx4 %10, %20, off offset:64\n\t"
        "global_load_dwordx4 %11, %20, off offset:128\n\t"
        "global_load_dwordx4 %12, %20, off offset:192\n\t"
        "global_load_dwordx4 %13, %20, off offset:256\n\t"
        "global_load_dwordx4 %14, %20, off offset:320"
        : "=&v"(tb[0]), "=&v"(tb[1]), "=&v"(tb[2]), "=&v"(tb[3]), "=&v"(tb[4]), "=&v"(tb[5]), "=&v"(tb[6]), "=&v"(tb[7]), "=&v"(lenr),
          "=&v"(qr[0]), "=&v"(qr[1]), "=&v"(qr[2]), "=&v"(qr[3]), "=&v"(qr[4]), "=&v"(qr[5])
        : "v"(lane * 16), "v"(lane * 16 + 4096), "s"(p.bt), "v"(r8 * 4), "s"(p.lens), "v"(qp)
        : "memory");
  }

  unsigned voff[6];
#pragma unroll
  for (int i = 0; i < 6; ++i) {
    const int pp = i * 64 + lane, r_ = (pp * 2731) >> 16, ch = pp - 24 * r_;
    voff[i] = (unsigned)(r_ * ROWB + (ch ^ (r_ & 6)) * 16 - (i & 3) * 1024);
  }
  if (tid < 9) *(LAS unsigned*)(lds + L_SYNC + tid * 4) = 0u;
  const unsigned ring = (unsigned)(size_t)(lds + L_RING) + (wave & 3) * R * TILE;
  const char* kh = p.k + h * (D * 2);
  const char* vh = p.v + h * (D * 2);
  asm volatile("" : "+v"(voff[0]), "+v"(voff[1]), "+v"(voff[2]), "+v"(voff[3]), "+v"(voff[4]), "+v"(voff[5]));
  asm volatile("s_waitcnt vmcnt(6)"
               : "+v"(tb[0]), "+v"(tb[1]), "+v"(tb[2]), "+v"(tb[3]), "+v"(tb[4]), "+v"(tb[5]), "+v"(tb[6]), "+v"(tb[7]), "+v"(lenr));

  int P = NB;
  {
    u64 mk[4];
#pragma unroll
    for (int j = 0; j < 4; ++j) {
      const int mn = min(min(min(tb[0][j], tb[1][j]), min(tb[2][j], tb[3][j])), min(min(tb[4][j], tb[5][j]), min(tb[6][j], tb[7][j])));
      const int mx = max(max(max(tb[0][j], tb[1][j]), max(tb[2][j], tb[3][j])), max(max(tb[4][j], tb[5][j]), max(tb[6][j], tb[7][j])));
      mk[j] = __ballot(mn != mx);
    }
#pragma unroll
    for (int j = 0; j < 4; ++j) if (mk[j]) P = min(P, 4 * (int)__builtin_ctzll(mk[j]) + j);
  }
  const int lenv = (lenr <= 0 || lenr > LK) ? LK : lenr;
  const int np = (lenv + BS - 1) / BS;
  const int n = max(0, np - P);
  int sumn = n, maxnp = np;
  sumn += dpp<DPP_X1>(sumn); maxnp = max(maxnp, dpp<DPP_X1>(maxnp));
  sumn += dpp<DPP_X2>(sumn); maxnp = max(maxnp, dpp<DPP_X2>(maxnp));
  sumn += dpp<DPP_HMIRROR>(sumn); maxnp = max(maxnp, dpp<DPP_HMIRROR>(maxnp));
  const int S = min(P, rfl(maxnp));
  const int j0 = 8 * wave + b;
  int slot_issue = 0;
  int issued = 0;
  unsigned t_p0 = now32();
  if (wave < NSW && j0 < S) {
    const int e = j0 & 3;
    const unsigned ph = (unsigned)rdl(e == 0 ? tb[0][0] : e == 1 ? tb[0][1] : e == 2 ? tb[0][2] : tb[0][3], j0 >> 2);
    dma_tile(kh + (size_t)ph * PAGEB, ring, voff[0], voff[1], voff[2], voff[3], voff[4], voff[5]);
    issued = 1;
    slot_issue = TILE;
    if (j0 + 32 < S) {
      const unsigned ph1 = (unsigned)rdl(e == 0 ? tb[0][0] : e == 1 ? tb[0][1] : e == 2 ? tb[0][2] : tb[0][3], (j0 >> 2) + 8);
      dma_tile(kh + (size_t)ph1 * PAGEB, ring + TILE, voff[0], voff[1], voff[2], voff[3], voff[4], voff[5]);
      issued = 2;
      slot_issue = 2 * TILE;
    }
  }
  const int T = S + rfl(sumn);
  const int rsv = min(n, RSV), na = n - rsv;
  int E = na;
  E += dpp<DPP_SHR + 1>(E); E += dpp<DPP_SHR + 2>(E); E += dpp<DPP_SHR + 4>(E);
  E += S;
  const int LA = rdl(E, 7), rsv_b = rdl(rsv, b), na_b = rdl(na, b);

  if (wave < NSW) {
    const int nA = max(0, (LA - j0 + 31) >> 5), nw = nA + max(0, (rsv_b - wave + 3) >> 2);
    int ent;
    {
#pragma unroll
      for (int r = 0; r < B; ++r) *(LAS i32x4*)(lds + L_TAB + r * (NB * 4) + lane * 16) = tb[r];
      const int j = 32 * lane + j0;
      int dn = 0, base = S;
#pragma unroll
      for (int r = 1; r < B; ++r) { const int e = rdl(E, r - 1); if (j >= e) { dn = r; base = e; } }
      const bool fo = lane < nA, sh = j < S;
      const int row = fo ? (sh ? 0 : dn) : b, tag = fo ? (sh ? 8 : dn) : b;
      int col = fo ? (sh ? j : P + j - base) : P + na_b + (lane - nA) * NSW + wave;
      col = min(max(col, 0), NB - 1);
      const int phys = *(const LAS int*)(lds + L_TAB + (row * NB + col) * 4);
      ent = (int)(((unsigned)phys << 12) | (unsigned)(tag << 8) | (unsigned)col);
    }
    const int ntiles = 2 * nw;
    asm volatile("" : "+v"(ent));
    volatile LAS u32x2* pace = (volatile LAS u32x2*)(lds + L_PACE + wave * 8);
    volatile LAS unsigned* prog = (volatile LAS unsigned*)(lds + L_PROG + wave * 4);
    {
      constexpr unsigned kap = KAPPA, lead = LEAD, pc0 = PC0;
      if (issued == 0) t_p0 = now32();
      const int span = (int)((unsigned)t_entry + pc0 + (unsigned)(T * kap) / 1000u - t_p0);
      unsigned step_q = span > 0 && ntiles > 0 ? (unsigned)((float)span * 256.f * __builtin_amdgcn_rcpf((float)ntiles)) : 0u;
      *pace = u32x2{(t_p0 << 8) - lead * step_q, step_q};
      *prog = 0u;
    }
    int ik = issued, iv = 0, bend = min(KV1, nA);
    auto issue = [&]() {
      int it_ = issued >> 1, isv_ = issued & 1;
      if (issued < 2 * nA) {
        if (ik < bend) { it_ = ik++; isv_ = 0; }
        else { it_ = iv++; isv_ = 1; if (iv == bend) bend = min(bend + KVPH, nA); }
      }
      const unsigned e = (unsigned)rdl(ent, it_);
      const char* base = (isv_ ? vh : kh) + (size_t)(e >> 12) * PAGEB;
      dma_tile(base, ring + slot_issue, voff[0], voff[1], voff[2], voff[3], voff[4], voff[5]);
      ++issued;
      slot_issue += TILE; if (slot_issue == R * TILE) slot_issue = 0;
    };
    const int l0 = (int)(((unsigned)(size_t)(p.k + h * (D * 2)) >> 7) & 7);
    const bool slowh = ((3 - l0) & 7) < 3;
    const int reffA = slowh ? RAS : RAN, reffB = slowh ? RBS : RBN;
    const int rsw = 2 * nA - RSW;
    const int reffK = RK > 0 ? RK : reffA;
    constexpr bool pumpk = PUMPK != 0;
    constexpr int xkq = XKQ;
    bool behind = false;
    auto pump = [&](int done, int need, bool kph = false) {
      while (issued < min(ntiles, done + (done < rsw ? (kph && !behind ? reffK : reffA) : reffB))) {
        const u32x2 pc = *pace;
        const unsigned st = (unsigned)rfl((int)pc[1]), due = (unsigned)rfl((int)pc[0]) + (unsigned)issued * st;
        const int x = (int)((now32() << 8) - due);
        if (x < 0) {
          if (issued > need) break;
          __builtin_amdgcn_s_sleep(1);
          continue;
        }
        behind = xkq > 0 && x > (int)((st * (unsigned)xkq) >> 4);
        issue();
      }
    };
    pump(INI - 3, 0);
    asm volatile("s_waitcnt lgkmcnt(0)" ::: "memory");
    __builtin_amdgcn_s_barrier();
    wait_tiles(issued);
    asm volatile("" : "+v"(qr[0]), "+v"(qr[1]), "+v"(qr[2]), "+v"(qr[3]), "+v"(qr[4]), "+v"(qr[5]));
    const int lenc = c >= 8 ? 0 : lenv;
    bf16x8 qf[6];
    {
      const bool z = c >= 8 || lenr <= 0;
#pragma unroll
      for (int s = 0; s < 6; ++s) qf[s] = __builtin_bit_cast(bf16x8, u32x4{z ? 0u : qr[s][0], z ? 0u : qr[s][1], z ? 0u : qr[s][2], z ? 0u : qr[s][3]});
    }

    unsigned ka[2], va[4];
#pragma unroll
    for (int e = 0; e < 2; ++e) ka[e] = ring + (unsigned)(16 * (24 * c + ((4 * e + g) ^ (c & 6))));
    {
      const int row = 4 * g + (c >> 2), pq = lane & 3, x = (row >> 1) & 3;
#pragma unroll
      for (int nl = 0; nl < 4; ++nl) va[nl] = ring + (unsigned)(16 * 24 * row + 32 * (nl ^ x) + 16 * (pq >> 1) + 8 * (pq & 1));
    }

    float m = -INFINITY, l = 0.f;
    f32x4 acc[12];
#pragma unroll
    for (int i = 0; i < 12; ++i) acc[i] = f32x4{0.f, 0.f, 0.f, 0.f};

    auto dump_all = [&]() {
      const float ls = xrow_sum(l);
      LAS char* dst = lds + L_DUMP1 + wave * DUMP1_W + c * QSTR;
      if (c < B && c != b) {
#pragma unroll
        for (int i = 0; i < 12; ++i) *(LAS f32x4*)(dst + i * 64 + g * 16) = acc[i];
        if (g == 0) *(LAS f32x2*)(lds + L_STAT1 + wave * 64 + c * 8) = f32x2{m, ls};
      }
      asm volatile("s_waitcnt lgkmcnt(0)" ::: "memory");
      if (lane < B) __hip_atomic_fetch_add((LAS unsigned*)(lds + L_SYNC + lane * 4), 1u, __ATOMIC_RELAXED, __HIP_MEMORY_SCOPE_WORKGROUP);
    };

    int slot = 0;
    {
      constexpr float la0 = LA0;
      if (la0 > 0.f && nw > 0) {
        wait_tiles(issued - 1);
        const u32x2 pc = *pace;
        const unsigned a = (unsigned)rfl((int)pc[0]), st = (unsigned)rfl((int)pc[1]);
        const unsigned lim = (now32() << 8) - (unsigned)(la0 * (float)st);
        if ((int)(lim - a) > 0) *pace = u32x2{lim, st};
      }
    }
    LAS char* const pst0 = lds + L_PST + wave * (KVPH * PSTB);
    auto kstep = [&](int t, int item, int ps) {
      LAS char* const pst = pst0 + ps * PSTB;
      const unsigned e = (unsigned)rdl(ent, item);
      const int col = e & 255, tag = (e >> 8) & 15;
      wait_tiles(issued - t - 1);
      *prog = t + 1;
      bf16x8 kf[6];
#pragma unroll
      for (int s = 0; s < 6; ++s) kf[s] = *(const LAS bf16x8*)(size_t)(ka[s & 1] + slot + 128 * (s >> 1));
      __builtin_amdgcn_sched_barrier(0);
      f32x4 s0 = {0.f, 0.f, 0.f, 0.f}, s1 = {0.f, 0.f, 0.f, 0.f};
#pragma unroll
      for (int s = 0; s < 6; s += 2) {
        s0 = __builtin_amdgcn_mfma_f32_16x16x32_bf16(kf[s], qf[s], s0, 0, 0, 0);
        s1 = __builtin_amdgcn_mfma_f32_16x16x32_bf16(kf[s + 1], qf[s + 1], s1, 0, 0, 0);
      }
      slot += TILE; if (slot == R * TILE) slot = 0;
      pump(t + 1, t + 1, true);
      const int rem = ((tag == 8 || tag == c) ? lenc : 0) - col * BS - 4 * g;
      float x[4];
#pragma unroll
      for (int j = 0; j < 4; ++j) x[j] = j < rem ? (s0[j] + s1[j]) * C2 : -INFINITY;
      const float pm = xrow_max(fmaxf(fmaxf(x[0], x[1]), fmaxf(x[2], x[3])));
      const float mn = fmaxf(m, pm);
      const float alpha = mn > m ? __builtin_amdgcn_exp2f(m - mn) : 1.f;
      m = mn;
      const float msub = m == -INFINITY ? 0.f : m;
      float pr[4];
#pragma unroll
      for (int j = 0; j < 4; ++j) pr[j] = __builtin_amdgcn_exp2f(x[j] - msub);
      l = l * alpha + ((pr[0] + pr[1]) + (pr[2] + pr[3]));
      if (c < B) {
        *(LAS u32x2*)(pst + (g * 8 + c) * 8) = u32x2{pack_bf16(pr[0], pr[1]), pack_bf16(pr[2], pr[3])};
        if (g == 0) *(LAS float*)(pst + 256 + c * 4) = alpha;
      }
      if (pumpk) pump(t + 1, -1, true);
    };
    auto vstep = [&](int t, int ps) {
      LAS char* const pst = pst0 + ps * PSTB;
      const u32x2 pu = *(const LAS u32x2*)(pst + (g * 8 + r8) * 8);
      const float alpha = *(const LAS float*)(pst + 256 + r8 * 4);
      wait_tiles(issued - t - 1);
      *prog = t + 1;
      s16x4 vf[12];
#pragma unroll
      for (int nn = 0; nn < 12; ++nn)
        vf[nn] = __builtin_amdgcn_ds_read_tr16_b64_v4i16((LAS s16x4*)(size_t)(va[nn & 3] + slot + 128 * (nn >> 2)));
      if (__any(alpha != 1.f)) {
#pragma unroll
        for (int ii = 0; ii < 12; ++ii) acc[ii] *= alpha;
      }
      const s16x4 pf = __builtin_bit_cast(s16x4, u32x2{c < B ? pu[0] : 0u, c < B ? pu[1] : 0u});
      __builtin_amdgcn_sched_barrier(0);
#pragma unroll
      for (int nn = 0; nn < 12; ++nn) acc[nn] = __builtin_amdgcn_mfma_f32_16x16x16bf16_1k(vf[nn], pf, acc[nn], 0, 0, 0);
      slot += TILE; if (slot == R * TILE) slot = 0;
      pump(t + 1, t + 1);
    };
    {
      int t = 0;
#pragma unroll 1
      for (int b0 = 0; b0 < nA;) {
        const int b1 = min(b0 == 0 ? KV1 : b0 + KVPH, nA);
#pragma unroll 1
        for (int i = b0; i < b1; ++i, ++t) kstep(t, i, i - b0);
#pragma unroll 1
        for (int i = b0; i < b1; ++i, ++t) vstep(t, i - b0);
        b0 = b1;
      }
      dump_all();
#pragma unroll 1
      for (int i = nA; i < nw; ++i, t += 2) { kstep(t, i, 0); vstep(t + 1, 0); }
    }
    {
      const float ls = xrow_sum(l);
      LAS char* dst = lds + L_DUMP2 + wave * QSTR;
      if (c == b) {
#pragma unroll
        for (int i = 0; i < 12; ++i) *(LAS f32x4*)(dst + i * 64 + g * 16) = acc[i];
        if (g == 0) *(LAS f32x2*)(dst + 768) = f32x2{m, ls};
      }
      asm volatile("s_waitcnt lgkmcnt(0)" ::: "memory");
      if (lane == 0) __hip_atomic_fetch_add((LAS unsigned*)(lds + L_SYNC + 32), 1u, __ATOMIC_RELAXED, __HIP_MEMORY_SCOPE_WORKGROUP);
    }
  } else {
    asm volatile("s_waitcnt vmcnt(0) lgkmcnt(0)"
                 : "+v"(qr[0]), "+v"(qr[1]), "+v"(qr[2]), "+v"(qr[3]), "+v"(qr[4]), "+v"(qr[5]) : : "memory");
    if (lane == 0) *(volatile LAS u32x2*)(lds + L_TAG) = u32x2{(unsigned)(epoch + 1), (unsigned)((epoch + 1) >> 32)};
    asm volatile("s_waitcnt lgkmcnt(0)" ::: "memory");
    __builtin_amdgcn_s_barrier();
    const unsigned ptag16 = ((unsigned)epoch + 1u) & 0xffffu;
    constexpr unsigned period = PERIOD;
    constexpr float k1 = K1, x0 = X0;
    constexpr float kp = KP, ki = KI, kiup = KIUP, fl = FL, fx = FX, gx = GX, xlate = XLATE, tlate = TLATE, tlate0 = TLATE0, fs = FSLOPE, slew = SLEW, kpu = KPU, tfast = TFAST, kmin = KMIN, kmax = KMAX, x1max = X1MAX, latew = LATEW;
    constexpr unsigned fstop = FSTOP;
    const unsigned te = (unsigned)t_entry;
    const int nt_w = 2 * (max(0, (LA - 8 * (lane & 3) - b + 31) >> 5) + max(0, (rsv_b - (lane & 3) + 3) >> 2));
    int nt_all = nt_w;
    nt_all += dpp<DPP_X1>(nt_all); nt_all += dpp<DPP_X2>(nt_all);
    nt_all = rfl(nt_all);
    bool pacing = T >= 384 && nt_all >= 32 && period > 0;
    const bool sampler = blockIdx.x != 0 && (NSAMP == 64 ? (b & 3) == ((h >> 2) & 3) : NSAMP == 32 ? b == (h >> 2) : (h < 16 && b == ((h * 5) & 7)));
    const int slot = NSAMP == 64 ? (b >> 2) * 32 + h : h;
    const unsigned stag16 = ptag16;
    char* slots = p.ws + WS_PROG;
    const int stopc = (nt_all * (int)fstop) >> 8;
    float lnA = 0.f, lnS = 0.f, S0 = 0.f, t_base = -1.f, l_base = 0.f, t_next = (float)TBASE1;
    if (blockIdx.x == 0) {
      const u32x2 pc = *(volatile LAS u32x2*)(lds + L_PACE);
      const float n0 = (float)rfl(nt_w);
      lnA = (float)(int)(pc[0] - (te << 8)) * (1.f / 256.f);
      lnS = (float)pc[1] * n0 * (1.f / 256.f);
      S0 = lnS;
    }
    const float st0 = (float)T * (float)(KAPPA * 256e-3f) * __builtin_amdgcn_rcpf((float)nt_w), st_min = 0.8f * st0, st_max = 1.3f * st0;
    unsigned seq = 0;
    bool first = true;
    constexpr float tbad = TBAD, lbad = LBAD, kmax2 = KMAX2, lmbad = LMBAD;
    float kcap = kmax, gxe = GX0;
    const float nt0 = (float)rfl(nt_w), na0 = (float)max(0, (LA - b + 31) >> 5);
    constexpr float dkph = DKPH;
    u64 y_last = 0;
    bool vtouched = false;
    unsigned next_t = te + TFIRST;
    while (pacing) {
      {
        const u32x4 pr = *(volatile LAS u32x4*)(lds + L_PROG);
        const int cons = rfl((int)(pr[0] + pr[1] + pr[2] + pr[3]));
        const unsigned t = now32();
        if (cons >= stopc) pacing = false;
        else if ((int)(t - next_t) >= 0) {
          next_t = t + period;
          if (VPF > 0 && !vtouched && (int)(t - te) >= VPFT) {
            vtouched = true;
            const size_t stride = (p.kvb / (32 * (VPF > 0 ? VPF : 1))) & ~(size_t)63;
            if (blockIdx.x != 0 && lane < VPF) (void)*(const volatile GAS unsigned*)(p.v + (size_t)(((blockIdx.x >> 3) & 31) * VPF + lane) * stride);
          }
          u64 y = 0;
          if (blockIdx.x == 0) {
            u64 x = 0;
            if (lane < NSAMP) x = __hip_atomic_load((GAS u64*)(slots + lane * 256), __ATOMIC_RELAXED, __HIP_MEMORY_SCOPE_AGENT);
            const bool ok = lane < NSAMP && (unsigned)(x >> 48) == stag16;
            const int nok = __builtin_popcountll(__ballot(ok));
            const unsigned t2 = now32();
            float dt = ok ? (float)(short)((unsigned)(x >> 32) - t2) : 0.f;
            float lg = ok ? ((float)((unsigned)x & 0xffffu) - 16384.f) * (1.f / 4096.f) : 0.f, cn = ok ? 1.f : 0.f;
            dt = wave_sum(dt);
            float late = 0.f;
            if (first) late = wave_sum(ok ? (float)(short)(((unsigned)x >> 16) - (te + (unsigned)(int)(lnA + lnS))) : 0.f);
            lg += dppf<DPP_ROR + 8>(lg); cn += dppf<DPP_ROR + 8>(cn);
            lg = xrow_sum(lg); cn = xrow_sum(cn);
            lg = cn > 0.f ? lg * __builtin_amdgcn_rcpf(cn) : 0.f;
            float lmax = lg, lmean = lg;
            lmax = fmaxf(lmax, dppf<DPP_X1>(lmax)); lmax = fmaxf(lmax, dppf<DPP_X2>(lmax)); lmax = fmaxf(lmax, dppf<DPP_HMIRROR>(lmax));
            lmean += dppf<DPP_X1>(lmean); lmean += dppf<DPP_X2>(lmean); lmean += dppf<DPP_HMIRROR>(lmean); lmean *= 0.125f;
            const float tnow = (float)(int)(t2 - te);
            bool send = false;
            float x_dbg = 0.f, d_dbg = 0.f;
            if (nok >= NSAMP * 3 / 4) {
              const float tm = tnow + dt * __builtin_amdgcn_rcpf((float)nok);
              float Sn = lnS;
              if (tm >= tbad && (lmax > lbad || lmean > lmbad)) { kcap = kmax2; gxe = gx; }
              const float s_lo = (float)T * (kmin * 1e-3f), s_hi = ((float)T * (kcap * 1e-3f) + (float)SOFF) * (1.f + xlate * fminf(fmaxf((tm - tlate0) * __builtin_amdgcn_rcpf(tlate - tlate0), 0.f), 1.f));
              if (first) {
                if (t_base < 0.f) { if (tm >= (float)TBASE1) { t_base = tm; l_base = lmean; } }
                else if (tm >= (float)TD1) {
                  const float d = (lmean - l_base) * 100.f * __builtin_amdgcn_rcpf(tm - t_base);
                  const float x = fminf(fmaxf(-d * k1 - x0, X1MIN), x1max);
                  Sn = S0 = fmaxf(lnS * __builtin_amdgcn_rcpf(1.f + x), s_lo);
                  lnA += latew * fmaxf(late * __builtin_amdgcn_rcpf((float)nok), 0.f);
                  first = false; send = true;
                  t_next = tnow + (float)TSW; t_base = tm; l_base = 0.f;
                  x_dbg = x; d_dbg = d;
                }
              } else if (tm >= t_next) {
                if (lnA + lnS - tm > (float)(REM_MIN * 100)) {
                  const float ps_ = (tm - lnA) * __builtin_amdgcn_rcpf(lnS) * nt0;
                  const float bk_ = ps_ < 2.f * KV1 ? 0.f : floorf((ps_ - 2.f * KV1) * (0.5f / KVPH));
                  const float i0_ = ps_ < 2.f * KV1 ? 0.f : KV1 + bk_ * KVPH;
                  const float ov_ = (ps_ - 2.f * i0_ - fminf(ps_ < 2.f * KV1 ? (float)KV1 : (float)KVPH, na0 - i0_)) * (1.f / DKW);
                  const float dk_ = ps_ < 2.f * na0 ? dkph * fminf(fmaxf(1.f - ov_, 0.f), 1.f) : 0.f;
                  const float e0_ = lmean - fl + fs * fmaxf((float)TAPER - (lnA + lnS - tm) * 0.01f, 0.f);
                  const float em = e0_ > 0.f ? fmaxf(e0_ - dk_, 0.f) : e0_;
                  const float e = tm >= (float)TGUARD && gxe > 0.f ? fmaxf(em, (lmax - fx - dk_) * gxe) : em;
                  const float dt_us = (tm - t_base) * 0.01f;
                  const float xb = l_base - (e > 0.f ? ki : kiup) * e * dt_us;
                  t_base = tm;
                  const float x = xb - (e > 0.f ? kp : kpu) * e;
                  Sn = S0 * __builtin_amdgcn_rcpf(1.f + x);
                  const float s_sl = tm < tfast ? 0.f : lnS * (1.f - slew * dt_us);
                  if (e > 0.f ? Sn < s_hi : Sn > s_lo && Sn > s_sl) l_base = xb;
                  Sn = fmaxf(Sn, s_sl);
                  send = true;
                  x_dbg = x; d_dbg = e;
                } else pacing = false;
              }
              if (send) {
                Sn = fminf(fmaxf(Sn, s_lo), s_hi);
                lnA = tnow - (tnow - lnA) * Sn * __builtin_amdgcn_rcpf(lnS);
                lnS = Sn;
              }
            }
            if (send) {
              y = ((u64)ptag16 << 48) | ((u64)(++seq & 0xffffu) << 32) | ((unsigned)(int)fminf(fmaxf(lmean * 4096.f, 0.f), 65535.f) << 16) | ((te + (unsigned)(int)(lnA + lnS)) & 0xffffu);
              if (lane < H) __hip_atomic_store((GAS u64*)(p.ws + WS_LINE + lane * 256), y, __ATOMIC_RELAXED, __HIP_MEMORY_SCOPE_AGENT);
            }
          } else {
            y = __hip_atomic_load((GAS u64*)(p.ws + WS_LINE + h * 256), __ATOMIC_RELAXED, __HIP_MEMORY_SCOPE_AGENT);
          }
          y = ((u64)(unsigned)rfl((int)(y >> 32)) << 32) | (unsigned)rfl((int)y);
          const bool fresh = (unsigned)(y >> 48) == (unsigned)rfl((int)ptag16) && y != y_last;
          if (fresh) y_last = y;
          asm volatile("" : "+v"(y_last) : : "memory");
          const u32x4 pr2 = *(volatile LAS u32x4*)(lds + L_PROG);
          const u32x2 pc = *(volatile LAS u32x2*)(lds + L_PACE + (lane & 3) * 8);
          const unsigned t2 = now32();
          const float pos = (float)(int)((t2 << 8) - pc[0]) * __builtin_amdgcn_rcpf((float)pc[1]);
          float due = fminf(pos, (float)nt_w);
          due += dppf<DPP_X1>(due); due += dppf<DPP_X2>(due);
          const float lag4 = due - (float)(int)(pr2[0] + pr2[1] + pr2[2] + pr2[3]);
          if (y_last != 0) {
            const float left = (float)(short)((unsigned)y_last - t2) * 256.f;
            const float rest = (float)nt_w - pos;
            if (fresh && lane < NSW && rest > 2.f && left > 0.f) {
              const float st = fminf(fmaxf(left * __builtin_amdgcn_rcpf(rest), st_min), st_max);
              *(volatile LAS u32x2*)(lds + L_PACE + lane * 8) = u32x2{(t2 << 8) - (unsigned)(int)(pos * st), (unsigned)st};
            }
          }
          if (sampler) {
            const float lag = fminf(fmaxf(lag4 * 1024.f + 16384.f, 0.f), 65535.f);
            const unsigned dend = ((pc[0] + (unsigned)nt_w * pc[1]) >> 8) & 0xffffu;
            if (lane == 0)
              __hip_atomic_store((GAS u64*)(slots + slot * 256), ((u64)((ptag16 << 16) | (t2 & 0xffffu)) << 32) | (dend << 16) | (unsigned)lag, __ATOMIC_RELAXED, __HIP_MEMORY_SCOPE_AGENT);
          }
        }
      }
      __builtin_amdgcn_s_sleep(4);
    }
  }
}

}

extern "C" void kernel_launch(void* const* d_in, const int* in_sizes, int n_in, void* d_out, int out_size, void* d_ws, size_t ws_size,
                              hipStream_t stream) {
  (void)hipFuncSetAttribute((const void*)paged_attn_kernel, hipFuncAttributeMaxDynamicSharedMemorySize, LDS_BYTES);
  Params p;
  p.bt = (const int*)d_in[3];
  p.lens = (const int*)d_in[4];
  p.q = (const unsigned short*)d_in[0];
  p.k = (const char*)d_in[1];
  p.v = (const char*)d_in[2];
  p.out = (unsigned short*)d_out;
  p.ws = (char*)d_ws;
  p.kvb = (unsigned long long)in_sizes[1] * 2;
  p.pad = 0;
  hipLaunchKernelGGL(paged_attn_kernel, dim3(H * B), dim3(NTHR), LDS_BYTES, stream, p);
}
```
